```python
import torch

def _softmax(x, axis=-1):
    return torch.softmax(x, dim=axis)
def _gelu(x):
    return torch.nn.functional.gelu(x, approximate="tanh")
def _silu(x):
    return torch.nn.functional.silu(x)
def _sigmoid(x):
    return torch.sigmoid(x)
def _layernorm(x, axis=-1, eps=1e-5):
    m = x.mean(dim=axis, keepdim=True)
    v = x.var(dim=axis, keepdim=True, unbiased=False)
    return (x - m) / torch.sqrt(v + eps)
def _scatter_add(tgt, idx, src, axis=0):
    return tgt.clone().index_add_(axis, idx, src)
def _segment_sum(src, seg, n):
    out = torch.zeros((n,) + src.shape[1:], dtype=src.dtype)
    return out.index_add_(0, seg, src)
def _pad(x, pw):
    flat = tuple(v for pair in reversed(pw) for v in pair)
    return torch.nn.functional.pad(x, flat)

kernel_name = 'interleave_layers__linear_attn_block__mha_block__prompt'

def setup_inputs():

    _w = (lambda a: (lambda t: t.to(torch.bfloat16) if t.is_floating_point() else t)(torch.as_tensor(a)))
    def _A_setup():
        B, S, D = 1, 2048, 576
        return {
            "x": _w((lambda *s: torch.randn(*s, dtype=torch.bfloat16) * 0.05)(B, S, D)),
            "wqkv": _w((lambda *s: torch.randn(*s, dtype=torch.bfloat16) * 0.05)(D, 3 * D)),
            "wo": _w((lambda *s: torch.randn(*s, dtype=torch.bfloat16) * 0.05)(D, D)),
        }
    _A_in = _A_setup()

    def _B_setup():
        B, S, D, H = 1, 2048, 576, 32
        return {
            "x": _w((lambda *s: torch.randn(*s, dtype=torch.bfloat16) * 0.05)(B, S, D)),
            "wqkv": _w((lambda *s: torch.randn(*s, dtype=torch.bfloat16) * 0.1)(D, 3 * D)),
            "wo": _w((lambda *s: torch.randn(*s, dtype=torch.bfloat16) * 0.05)(D, D)),
        }
    _B_in = _B_setup()

    return {
        "A_x": _A_in["x"],
        "A_wqkv": _A_in["wqkv"],
        "A_wo": _A_in["wo"],
        "B_x": _B_in["x"],
        "B_wqkv": _B_in["wqkv"],
        "B_wo": _B_in["wo"],
    }

def reference(A_x, A_wqkv, A_wo, B_x, B_wqkv, B_wo):
    def _A_ref(x, wqkv, wo):
        B, S, D = x.shape
        H = 32; Dh = D // H
        qkv = (lambda x, *d: x.permute(*d))((x @ wqkv).reshape(B, S, 3, H, Dh), 2, 0, 3, 1, 4)
        q, k, v = qkv[0], qkv[1], qkv[2]
        phi = lambda t: torch.where(t > 0, t + 1, torch.exp(t)) + 1e-6
        pq, pk = phi(q), phi(k)
        kv = torch.einsum("bhsd,bhse->bhde", pk, v)
        z = torch.einsum("bhsd->bhd", pk)
        num = torch.einsum("bhqd,bhde->bhqe", pq, kv)
        den = torch.einsum("bhqd,bhd->bhq", pq, z)[..., None] + 1e-6
        o = num / den
        return (lambda x, *d: x.permute(*d))(o, 0, 2, 1, 3).reshape(B, S, D) @ wo

    def _B_ref(x, wqkv, wo):
        B, S, D = x.shape
        H = 32
        Dh = D // H
        qkv = (lambda x, *d: x.permute(*d))((x @ wqkv).reshape(B, S, 3, H, Dh), 2, 0, 3, 1, 4)
        q, k, v = qkv[0], qkv[1], qkv[2]
        scores = torch.einsum("bhqd,bhkd->bhqk", q, k) * (Dh ** -0.5)
        p = _softmax(scores, axis=-1)
        o = torch.einsum("bhqk,bhkd->bhqd", p, v)
        return (lambda x, *d: x.permute(*d))(o, 0, 2, 1, 3).reshape(B, S, D) @ wo

    x = A_x
    x = x + _A_ref(_layernorm(x, axis=-1), A_wqkv, A_wo)
    x = x + _B_ref(_layernorm(x, axis=-1), B_wqkv, B_wo)
    x = x + _A_ref(_layernorm(x, axis=-1), A_wqkv, A_wo)
    x = x + _B_ref(_layernorm(x, axis=-1), B_wqkv, B_wo)
    return x
```

```cpp
#include <hip/hip_runtime.h>
#include <hip/hip_bf16.h>
#include <stdint.h>

typedef __attribute__((ext_vector_type(8))) short bf16x8;
typedef __attribute__((ext_vector_type(4))) short bf16x4;
typedef __attribute__((ext_vector_type(4))) float f32x4;
typedef __attribute__((ext_vector_type(2))) float f32x2;
typedef __attribute__((ext_vector_type(16))) float f32x16;
typedef __attribute__((ext_vector_type(4))) unsigned u32x4;
typedef __attribute__((ext_vector_type(2))) unsigned u32x2;
typedef __attribute__((ext_vector_type(2))) __bf16 bf16x2_t;

#define DI __device__ __forceinline__

constexpr int S_ = 2048, D_ = 576, H_ = 32, DH = 18, N3 = 1728;

constexpr size_t WT_BYTES   = 16 * 126 * 1024 + 4096;
constexpr size_t WO_WAVE    = 20 * 1024 + 5 * 512;
constexpr size_t WO_BYTES   = 8 * 4 * WO_WAVE;
constexpr size_t CS_BYTES   = 6 * 16 * 112 * 4;
constexpr size_t XT_BYTES   = (size_t)S_ * D_ * 2;
constexpr size_t ST_BYTES   = 8 * S_ * 8;
constexpr size_t KVB_FLOATS = 32 * 19 * 20;
constexpr size_t KVB_BYTES  = KVB_FLOATS * 4;
constexpr size_t IMG_GROUP  = 9472;
constexpr size_t IMG_HEAD   = 16 * IMG_GROUP;
constexpr size_t AL(size_t x) { return (x + 4095) & ~(size_t)4095; }
constexpr size_t OFF_WTA = 0;
constexpr size_t OFF_WTB = OFF_WTA + AL(WT_BYTES);
constexpr size_t OFF_WOA = OFF_WTB + AL(WT_BYTES);
constexpr size_t OFF_WOB = OFF_WOA + AL(WO_BYTES);
constexpr size_t OFF_CSA = OFF_WOB + AL(WO_BYTES);
constexpr size_t OFF_CSB = OFF_CSA + AL(CS_BYTES);
constexpr size_t OFF_XT  = OFF_CSB + AL(CS_BYTES);
constexpr size_t OFF_ST  = OFF_XT + 4 * AL(XT_BYTES);
constexpr size_t OFF_KVB = OFF_ST + 4 * AL(ST_BYTES);
constexpr size_t OFF_QM  = OFF_KVB + 2 * AL(KVB_BYTES);
constexpr size_t OFF_QX  = OFF_QM + AL((size_t)S_ * 32 * 32);
constexpr size_t OFF_IMG = OFF_QX + AL((size_t)S_ * 32 * 4);
constexpr size_t OFF_OM  = OFF_IMG + AL(32 * IMG_HEAD);
constexpr size_t OFF_OX  = OFF_OM + AL((size_t)S_ * 32 * 32);
constexpr size_t WS_TOTAL = OFF_OX + AL((size_t)S_ * 32 * 4);

DI unsigned pk2(float a, float b) {
    f32x2 v = {a, b};
    bf16x2_t r = __builtin_convertvector(v, bf16x2_t);
    return __builtin_bit_cast(unsigned, r);
}
DI float bflo(unsigned u) { return __uint_as_float(u << 16); }
DI float bfhi(unsigned u) { return __uint_as_float(u & 0xffff0000u); }
DI f32x4 mfma16(bf16x8 a, bf16x8 b, f32x4 c) { return __builtin_amdgcn_mfma_f32_16x16x32_bf16(a, b, c, 0, 0, 0); }
DI f32x4 mfma16k16(bf16x4 a, bf16x4 b, f32x4 c) { return __builtin_amdgcn_mfma_f32_16x16x16bf16_1k(a, b, c, 0, 0, 0); }
DI f32x16 mfma32(bf16x8 a, bf16x8 b, f32x16 c) { return __builtin_amdgcn_mfma_f32_32x32x16_bf16(a, b, c, 0, 0, 0); }
DI f32x16 mfma32k8(bf16x4 a, bf16x4 b, f32x16 c) { return __builtin_amdgcn_mfma_f32_32x32x8bf16_1k(a, b, c, 0, 0, 0); }
DI bf16x8 asbf8(u32x4 v) { return __builtin_bit_cast(bf16x8, v); }
DI bf16x4 asbf4(u32x2 v) { return __builtin_bit_cast(bf16x4, v); }
DI float phi_f(float t) { return (t > 0.f ? t + 1.0f : __expf(t)) + 1e-6f; }
DI int vpos(int d) { return d >= 18 ? 4 : (d == 4 ? 18 : d); }
DI int swap34(int i) { return (i & 7) | ((i & 8) << 1) | ((i & 16) >> 1); }

DI int qkv_col(bool isA, int nt, int ct, int r) {
    const int ha = 2 * nt;
    if (ct < 2 || (!isA && ct < 4)) {
        const int gp = r >> 2, i = r & 3;
        const int base = (ct < 2) ? 0 : 576;
        return base + (ha + (gp >> 1)) * 18 + 8 * (gp & 1) + 4 * (ct & 1) + i;
    }
    if (ct < 4) return 576 + (ha + (ct & 1)) * 18 + r;
    if (ct < 6) return 1152 + (ha + (ct & 1)) * 18 + r;
    if (r >= 12) return -1;
    return (r >> 2) * 576 + (ha + ((r >> 1) & 1)) * 18 + 16 + (r & 1);
}

constexpr int PREP_W_BLOCKS = 192, PREP_WO_BLOCKS = 64, PREP_X_BLOCKS = 64, PREP_Z_BLOCKS = 2;
__global__ __launch_bounds__(256) void prep_kernel(const uint16_t* __restrict__ x0, const uint16_t* __restrict__ wA,
                                                   const uint16_t* __restrict__ woA, const uint16_t* __restrict__ wB,
                                                   const uint16_t* __restrict__ woB, char* __restrict__ ws) {
    __shared__ float red[4][112];
    const int tid = threadIdx.x, lane = tid & 63, w = tid >> 6;
    int b = blockIdx.x;
    if (b < PREP_W_BLOCKS) {
        const int wsel = b / 96, rem = b % 96, nt = rem / 6, kg = rem % 6;
        const uint16_t* W = wsel ? wB : wA;
        u32x4* WT = (u32x4*)(ws + (wsel ? OFF_WTB : OFF_WTA));
        float* CS = (float*)(ws + (wsel ? OFF_CSB : OFF_CSA));
        float s0 = 0.f, s1 = 0.f;
        const int ct = lane >> 3, r = (lane & 7) * 2;
        if (lane < 56) {
            const int col = qkv_col(wsel == 0, nt, ct, r);
#pragma unroll
            for (int q = 0; q < 3; ++q) {
                const int ko = kg * 12 + w * 3 + q;
                unsigned v[8];
#pragma unroll
                for (int j = 0; j < 8; ++j)
                    v[j] = (col >= 0) ? *(const unsigned*)(W + (size_t)(ko * 8 + j) * N3 + col) : 0u;
                u32x4 lo, hi;
#pragma unroll
                for (int j = 0; j < 4; ++j) {
                    lo[j] = (v[2 * j] & 0xffffu) | (v[2 * j + 1] << 16);
                    hi[j] = (v[2 * j] >> 16) | (v[2 * j + 1] & 0xffff0000u);
                }
#pragma unroll
                for (int j = 0; j < 8; ++j) { s0 += bflo(v[j]); s1 += bfhi(v[j]); }
                const int ks = ko >> 2, g = ko & 3;
                const size_t idx = ((size_t)(nt * 18 + ks) * 7 + ct) * 64 + g * 16 + r;
                WT[idx] = lo; WT[idx + 1] = hi;
            }
            red[w][ct * 16 + r] = s0; red[w][ct * 16 + r + 1] = s1;
        }
        __syncthreads();
        if (tid < 112) CS[(kg * 16 + nt) * 112 + tid] = (red[0][tid] + red[1][tid]) + (red[2][tid] + red[3][tid]);
        return;
    }
    b -= PREP_W_BLOCKS;
    if (b < PREP_WO_BLOCKS) {
        const int wsel = b >> 5, nb = (b >> 2) & 7, wv = b & 3;
        const uint16_t* W = wsel ? woB : woA;
        char* dst = ws + (wsel ? OFF_WOB : OFF_WOA) + (size_t)(nb * 4 + wv) * WO_WAVE;
        for (int task = tid; task < 576 + 144; task += 256) {
            const bool ex = task >= 576;
            const int tt = ex ? task - 576 : task;
            const int cp = tt % 36, kq = tt / 36;
            const int c = cp * 2, cc = c >> 3, pos = c & 7;
            int t, r;
            if (cc < 8) { t = 2 * (cc >> 2) + (pos >> 2); r = 4 * (cc & 3) + (pos & 3); } else { t = 4; r = pos; }
            const int col = nb * 72 + c;
            if (!ex) {
                const int ks = kq >> 2, g = kq & 3, P = 4 * wv + ks;
                unsigned v[8];
#pragma unroll
                for (int j = 0; j < 8; ++j) {
                    int row;
                    if (wsel) row = (2 * P + (g >> 1)) * 18 + 4 * (g & 1) + (j & 3) + 8 * (j >> 2);
                    else      row = (2 * P + (j >> 2)) * 18 + 4 * g + (j & 3);
                    v[j] = *(const unsigned*)(W + (size_t)row * D_ + col);
                }
                u32x4 lo, hi;
#pragma unroll
                for (int j = 0; j < 4; ++j) {
                    lo[j] = (v[2 * j] & 0xffffu) | (v[2 * j + 1] << 16);
                    hi[j] = (v[2 * j] >> 16) | (v[2 * j + 1] & 0xffff0000u);
                }
                u32x4* d = (u32x4*)(dst + (size_t)(ks * 5 + t) * 1024) + g * 16 + r;
                d[0] = lo; d[1] = hi;
            } else {
                const int g = kq;
                unsigned v[4];
#pragma unroll
                for (int j = 0; j < 4; ++j) {
                    const int row = (8 * wv + g + 4 * (j >> 1)) * 18 + 16 + (j & 1);
                    v[j] = *(const unsigned*)(W + (size_t)row * D_ + col);
                }
                u32x2 lo, hi;
#pragma unroll
                for (int j = 0; j < 2; ++j) {
                    lo[j] = (v[2 * j] & 0xffffu) | (v[2 * j + 1] << 16);
                    hi[j] = (v[2 * j] >> 16) | (v[2 * j + 1] & 0xffff0000u);
                }
                u32x2* d = (u32x2*)(dst + 20480 + (size_t)t * 512) + g * 16 + r;
                d[0] = lo; d[1] = hi;
            }
        }
        return;
    }
    b -= PREP_WO_BLOCKS;
    if (b < PREP_X_BLOCKS) {
        const int idx = b * 256 + tid, row = idx >> 3, part = idx & 7;
        const u32x4* src = (const u32x4*)(x0 + (size_t)row * D_ + part * 72);
        u32x4* XT = (u32x4*)(ws + OFF_XT);
        u32x4 v[9];
#pragma unroll
        for (int c = 0; c < 9; ++c) v[c] = src[c];
        float s = 0.f;
#pragma unroll
        for (int c = 0; c < 9; ++c)
#pragma unroll
            for (int j = 0; j < 4; ++j) s += bflo(v[c][j]) + bfhi(v[c][j]);
        const float mu = s * (1.0f / 72.0f);
        float m2 = 0.f;
#pragma unroll
        for (int c = 0; c < 9; ++c)
#pragma unroll
            for (int j = 0; j < 4; ++j) { float a = bflo(v[c][j]) - mu, bb = bfhi(v[c][j]) - mu; m2 += a * a + bb * bb; }
#pragma unroll
        for (int c = 0; c < 9; ++c) {
            const int cidx = part * 9 + c;
            XT[((size_t)(row >> 4) * 18 + (cidx >> 2)) * 64 + (cidx & 3) * 16 + (row & 15)] = v[c];
        }
        ((f32x2*)(ws + OFF_ST))[part * S_ + row] = (f32x2){s, m2};
        return;
    }
    b -= PREP_X_BLOCKS;
    {
        u32x4* z = (u32x4*)(ws + OFF_KVB + (size_t)b * AL(KVB_BYTES));
        for (int i = tid; i < (int)(KVB_BYTES / 16); i += 256) z[i] = (u32x4){0, 0, 0, 0};
    }
}

template <bool IS_A>
__global__ __launch_bounds__(256, 1) void qkv_kernel(const u32x4* __restrict__ XT, const f32x2* __restrict__ stats,
                                                     const u32x4* __restrict__ WT, const float* __restrict__ CS,
                                                     char* __restrict__ QM, char* __restrict__ QX,
                                                     char* __restrict__ IMG, float* __restrict__ KVB) {
    __shared__ __attribute__((aligned(16))) char lds[131072 + 1024 + 512];
    f32x2* lstat = (f32x2*)(lds + 131072);
    float* lcs = (float*)(lds + 131072 + 1024);
    const int tid = threadIdx.x, lane = tid & 63, w = __builtin_amdgcn_readfirstlane(tid >> 6);
    const int r16 = lane & 15, g = lane >> 4;
    int mt, nt;
    { const int b = blockIdx.x, xcd = b & 7, i = b >> 3; mt = (xcd >> 1) * 4 + (i & 3); nt = (xcd & 1) * 8 + (i >> 2); }

    f32x2 pst[8]; float pcs[6];
    if (tid < 128) {
#pragma unroll
        for (int p = 0; p < 8; ++p) pst[p] = stats[p * S_ + mt * 128 + tid];
    } else if (tid < 240) {
#pragma unroll
        for (int p = 0; p < 6; ++p) pcs[p] = CS[(p * 16 + nt) * 112 + (tid - 128)];
    }

    const u32x4* wsrc = WT + (size_t)nt * (126 * 64) + tid;
    const u32x4* xsrc = XT + (size_t)(mt * 8 + 2 * w) * (18 * 64) + lane;
    u32x4 wst[32];
    u32x4 xf[2][18];
    {
        int p = 0;
#pragma unroll
        for (int ks = 0; ks < 18; ++ks) {
            const int need = ((ks + 1) * 7 + 3) / 4;
#pragma unroll
            for (int q = 0; q < 2; ++q) if (p < need) { wst[p] = wsrc[p * 256]; ++p; }
            xf[0][ks] = xsrc[ks * 64];
            xf[1][ks] = xsrc[(18 + ks) * 64];
        }
    }
    f32x4 acc[2][7];
#pragma unroll
    for (int a = 0; a < 2; ++a)
#pragma unroll
        for (int c = 0; c < 7; ++c) acc[a][c] = (f32x4){0.f, 0.f, 0.f, 0.f};

    u32x4* lw = (u32x4*)lds;
    int pw = 0;
#pragma unroll
    for (int grp = 0; grp < 6; ++grp) {
        const int need = ((grp + 1) * 21 + 3) / 4;
#pragma unroll
        for (int q = 0; q < 6; ++q) if (pw < need) { lw[pw * 256 + tid] = wst[pw]; ++pw; }
        __syncthreads();
#pragma unroll
        for (int k3 = 0; k3 < 3; ++k3) {
            const int ks = grp * 3 + k3;
            bf16x8 wf[7];
#pragma unroll
            for (int c = 0; c < 7; ++c) wf[c] = asbf8(lw[(ks * 7 + c) * 64 + lane]);
#pragma unroll
            for (int c = 0; c < 7; ++c) {
                const bool tr = (c < 2) || (!IS_A && c < 4);
#pragma unroll
                for (int a = 0; a < 2; ++a) {
                    if (tr) acc[a][c] = mfma16(wf[c], asbf8(xf[a][ks]), acc[a][c]);
                    else    acc[a][c] = mfma16(asbf8(xf[a][ks]), wf[c], acc[a][c]);
                }
            }
        }
    }

    if (tid < 128) {
        float s = 0.f;
#pragma unroll
        for (int p = 0; p < 8; ++p) s += pst[p][0];
        const float mean = s * (1.0f / 576.0f);
        float m2 = 0.f;
#pragma unroll
        for (int p = 0; p < 8; ++p) { const float d = pst[p][0] * (1.0f / 72.0f) - mean; m2 += pst[p][1] + 72.0f * d * d; }
        lstat[tid] = (f32x2){mean, __frsqrt_rn(m2 * (1.0f / 576.0f) + 1e-5f)};
    } else if (tid < 240) {
        lcs[tid - 128] = ((pcs[0] + pcs[1]) + (pcs[2] + pcs[3])) + (pcs[4] + pcs[5]);
    }
    __syncthreads();

    const int ha = 2 * nt;
    const int srow0 = mt * 128 + w * 32;
    f32x2 stT[2], stN[2][4];
#pragma unroll
    for (int a = 0; a < 2; ++a) {
        stT[a] = lstat[w * 32 + a * 16 + r16];
#pragma unroll
        for (int i = 0; i < 4; ++i) stN[a][i] = lstat[w * 32 + a * 16 + 4 * g + i];
    }
    auto pairvals = [&](int c0, int a, float (&v)[8]) {
#pragma unroll
        for (int i = 0; i < 4; ++i) {
            v[i]     = stT[a][1] * (acc[a][c0][i]     - stT[a][0] * lcs[c0 * 16 + 4 * g + i]);
            v[4 + i] = stT[a][1] * (acc[a][c0 + 1][i] - stT[a][0] * lcs[(c0 + 1) * 16 + 4 * g + i]);
        }
    };
    auto normvals = [&](int c, float (&v)[8]) {
        const float cs = lcs[c * 16 + r16];
#pragma unroll
        for (int a = 0; a < 2; ++a)
#pragma unroll
            for (int i = 0; i < 4; ++i) v[a * 4 + i] = stN[a][i][1] * (acc[a][c][i] - stN[a][i][0] * cs);
    };
    auto pack8 = [&](const float (&v)[8]) {
        return (u32x4){pk2(v[0], v[1]), pk2(v[2], v[3]), pk2(v[4], v[5]), pk2(v[6], v[7])};
    };

    if constexpr (!IS_A) {
        constexpr float QS = 0.23570226039551584f * 1.4426950408889634f;
        const int kt = srow0 >> 5;
        const int head = ha + (g >> 1), hh = g & 1;
        char* img = IMG + (size_t)head * IMG_HEAD + (size_t)(kt >> 2) * IMG_GROUP;
#pragma unroll
        for (int a = 0; a < 2; ++a) {
            float v[8];
            pairvals(0, a, v);
#pragma unroll
            for (int j = 0; j < 8; ++j) v[j] *= QS;
            *(u32x4*)(QM + ((((size_t)head * 64 + kt) * 2 + hh) * 32 + a * 16 + r16) * 16) = pack8(v);
            pairvals(2, a, v);
            *(u32x4*)(img + (kt & 3) * 1024 + hh * 512 + swap34(a * 16 + r16) * 16) = pack8(v);
        }
#pragma unroll
        for (int c = 4; c < 6; ++c) {
            float v[8];
            normvals(c, v);
            char* im2 = IMG + (size_t)(ha + (c & 1)) * IMG_HEAD + (size_t)(kt >> 2) * IMG_GROUP;
            *(u32x4*)(im2 + 4608 + (kt & 3) * 1216 + g * 304 + vpos(r16) * 16) = pack8(v);
        }
        {
            float v[8];
            normvals(6, v);
            const int hx = ha + ((r16 >> 1) & 1), dx = r16 & 1;
            char* im2 = IMG + (size_t)hx * IMG_HEAD + (size_t)(kt >> 2) * IMG_GROUP;
            if (r16 < 4) {
#pragma unroll
                for (int j = 0; j < 8; ++j) {
                    const int s = srow0 + (j >> 2) * 16 + 4 * g + (j & 3);
                    *(uint16_t*)(QX + ((size_t)hx * S_ + s) * 4 + dx * 2) = (uint16_t)(pk2(v[j] * QS, 0.f) & 0xffffu);
                }
            } else if (r16 < 8) {
#pragma unroll
                for (int j = 0; j < 8; ++j) {
                    const int row = swap34((j >> 2) * 16 + 4 * g + (j & 3));
                    *(uint16_t*)(im2 + 4096 + row * 16 + (kt & 3) * 4 + dx * 2) = (uint16_t)(pk2(v[j], 0.f) & 0xffffu);
                }
            } else if (r16 < 12) {
                *(u32x4*)(im2 + 4608 + (kt & 3) * 1216 + g * 304 + (16 + dx) * 16) = pack8(v);
            } else if (r16 < 14) {
                char* im3 = IMG + (size_t)(ha + dx) * IMG_HEAD + (size_t)(kt >> 2) * IMG_GROUP;
                *(u32x4*)(im3 + 4608 + (kt & 3) * 1216 + g * 304 + 4 * 16) =
                    (u32x4){0x3f803f80u, 0x3f803f80u, 0x3f803f80u, 0x3f803f80u};
            }
        }
    } else {
        const int st0 = srow0 >> 4;
#pragma unroll
        for (int a = 0; a < 2; ++a) {
            float v[8];
            pairvals(0, a, v);
#pragma unroll
            for (int j = 0; j < 8; ++j) v[j] = phi_f(v[j]);
            *(u32x4*)(QM + ((size_t)(st0 + a) * 32 + ha) * 512 + lane * 16) = pack8(v);
        }
        bf16x8 PK[2], VV[2], EX;
#pragma unroll
        for (int c = 0; c < 2; ++c) {
            float v[8];
            normvals(2 + c, v);
#pragma unroll
            for (int j = 0; j < 8; ++j) v[j] = phi_f(v[j]);
            PK[c] = asbf8(pack8(v));
            normvals(4 + c, v);
            VV[c] = asbf8(pack8(v));
        }
        {
            float v[8];
            normvals(6, v);
#pragma unroll
            for (int j = 0; j < 8; ++j) {
                const float p = phi_f(v[j]);
                v[j] = r16 < 8 ? p : (r16 < 12 ? v[j] : (r16 == 12 ? 1.0f : 0.0f));
            }
            const u32x4 e = pack8(v);
            EX = asbf8(e);
            if (r16 < 4) {
                const int hx = ha + (r16 >> 1);
#pragma unroll
                for (int j = 0; j < 8; ++j) {
                    const unsigned u = e[j >> 1];
                    const uint16_t val = (uint16_t)((j & 1) ? (u >> 16) : (u & 0xffffu));
                    *(uint16_t*)(QX + (((size_t)(st0 + (j >> 2)) * 8 + (hx >> 2)) * 16 + 4 * g + (j & 3)) * 16 + (hx & 3) * 4 + (r16 & 1) * 2) = val;
                }
            }
        }
        const f32x4 z4 = {0.f, 0.f, 0.f, 0.f};
        f32x4 dt[7];
        dt[0] = mfma16(VV[0], PK[0], z4);
        dt[1] = mfma16(VV[1], PK[1], z4);
        dt[2] = mfma16(VV[0], EX, z4);
        dt[3] = mfma16(VV[1], EX, z4);
        dt[4] = mfma16(EX, PK[0], z4);
        dt[5] = mfma16(EX, PK[1], z4);
        dt[6] = mfma16(EX, EX, z4);
        f32x4* lr = (f32x4*)lds;
#pragma unroll
        for (int t = 0; t < 7; ++t) lr[(w * 7 + t) * 64 + lane] = dt[t];
        __syncthreads();
        const float* lf = (const float*)lds;
        for (int o = tid; o < 684; o += 256) {
            const int hb = o / 342, rem = o % 342, e = rem / 18, d = rem % 18;
            int t, row, col;
            const int erow = (e < 18) ? 8 + 2 * hb + (e - 16) : 12;
            const int dcol = 4 + 2 * hb + (d - 16);
            if (e < 16) { row = e; if (d < 16) { t = hb; col = d; } else { t = 2 + hb; col = dcol; } }
            else        { row = erow; if (d < 16) { t = 4 + hb; col = d; } else { t = 6; col = dcol; } }
            const int off = (t * 64 + (row >> 2) * 16 + col) * 4 + (row & 3);
            const float sum = (lf[off] + lf[off + 7 * 256]) + (lf[off + 14 * 256] + lf[off + 21 * 256]);
            atomicAdd(KVB + ((ha + hb) * 19 + e) * 20 + d, sum);
        }
    }
}

template <bool IS_A, bool FINAL>
__global__ __launch_bounds__(256, 1) void oproj_kernel(const char* __restrict__ AM, const char* __restrict__ AX,
                                                       const float* __restrict__ KVB, const char* __restrict__ WO,
                                                       const u32x4* __restrict__ Xold, u32x4* __restrict__ Xnew,
                                                       f32x2* __restrict__ stats, uint16_t* __restrict__ out) {
    __shared__ __attribute__((aligned(16))) f32x4 lred[4 * 4 * 5 * 64];
    const int tid = threadIdx.x, lane = tid & 63, w = __builtin_amdgcn_readfirstlane(tid >> 6);
    const int r16 = lane & 15, g = lane >> 4;
    int sb, nb;
    { const int b = blockIdx.x, xcd = b & 7, i = b >> 3; sb = xcd * 4 + (i & 3); nb = i >> 2; }
    const int st0 = sb * 4;

    const char* wsrc = WO + (size_t)(nb * 4 + w) * WO_WAVE;
    u32x4 wf[4][5]; u32x2 wx[5];
    u32x4 af[4][4];
    u32x2 ax[4];
    u32x4 pq[4][8];
    f32x4 kvr[12];
    const u32x4 zz = {0, 0, 0, 0};
    if constexpr (IS_A) {
        const f32x4* kb = (const f32x4*)(KVB + (size_t)(8 * w) * (19 * 20));
#pragma unroll
        for (int i = 0; i < 12; ++i) {
            const int idx = i * 64 + lane;
            kvr[i] = (idx < 760) ? kb[idx] : (f32x4){0.f, 0.f, 0.f, 0.f};
        }
#pragma unroll
        for (int h = 0; h < 8; ++h) {
            const int head = 8 * w + h;
#pragma unroll
            for (int s = 0; s < 4; ++s) {
                const char* pm = AM + ((size_t)(st0 + s) * 32 + head) * 512 + lane * 16;
                const char* px = AX + (((size_t)(st0 + s) * 8 + (head >> 2)) * 16 + r16) * 16;
                pq[s][h] = (g < 3) ? *(const u32x4*)(g < 2 ? pm : px) : zz;
            }
        }
    } else {
#pragma unroll
        for (int ks = 0; ks < 4; ++ks)
#pragma unroll
            for (int s = 0; s < 4; ++s)
                af[s][ks] = *(const u32x4*)(AM + ((size_t)(st0 + s) * 16 + 4 * w + ks) * 1024 + lane * 16);
#pragma unroll
        for (int s = 0; s < 4; ++s) ax[s] = *(const u32x2*)(AX + ((size_t)(st0 + s) * 4 + w) * 512 + lane * 8);
    }
#pragma unroll
    for (int ks = 0; ks < 4; ++ks)
#pragma unroll
        for (int t = 0; t < 5; ++t)
            wf[ks][t] = (t < 4 || r16 < 8) ? *(const u32x4*)(wsrc + (ks * 5 + t) * 1024 + lane * 16) : zz;
#pragma unroll
    for (int t = 0; t < 5; ++t)
        wx[t] = (t < 4 || r16 < 8) ? *(const u32x2*)(wsrc + 20480 + t * 512 + lane * 8) : (u32x2){0, 0};
    const int stw = st0 + w;
    const int c0 = nb * 9 + g, c1 = nb * 9 + 4 + g, c2 = nb * 9 + 8;
    const size_t i0 = ((size_t)stw * 18 + (c0 >> 2)) * 64 + (c0 & 3) * 16 + r16;
    const size_t i1 = ((size_t)stw * 18 + (c1 >> 2)) * 64 + (c1 & 3) * 16 + r16;
    const size_t i2 = ((size_t)stw * 18 + (c2 >> 2)) * 64 + (c2 & 3) * 16 + r16;
    const u32x4 xo0 = Xold[i0], xo1 = Xold[i1];
    const u32x2 xo2 = (g < 2) ? ((const u32x2*)(Xold + i2))[g] : (u32x2){0, 0};

    const f32x4 z4 = {0.f, 0.f, 0.f, 0.f};
    if constexpr (IS_A) {
        char* lkv = (char*)(lred + w * (4 * 5 * 64));
#pragma unroll
        for (int i = 0; i < 12; ++i) {
            const int idx = i * 64 + lane;
            if (idx < 760) {
                const int row = idx / 5, c = idx - row * 5;
                *(u32x2*)(lkv + row * 48 + c * 8) = (u32x2){pk2(kvr[i][0], kvr[i][1]), pk2(kvr[i][2], kvr[i][3])};
            }
        }
        const int goff = (g < 2 ? g : 2) * 16, i4 = r16 & 3, gp = r16 >> 2;
#pragma unroll
        for (int s = 0; s < 4; ++s) ax[s] = (u32x2){0, 0};
#pragma unroll
        for (int h = 0; h < 8; ++h) {
            u32x4 a0 = *(const u32x4*)(lkv + (h * 19 + r16) * 48 + goff);
            const bool own = (gp == (h & 3)) && (i4 < 2);
            const int e1 = (i4 == 3) ? 18 : (own ? 16 + i4 : 0);
            u32x4 a1 = *(const u32x4*)(lkv + (h * 19 + e1) * 48 + goff);
            if (!(i4 == 3 || own)) a1 = zz;
            if (g >= 2) {
                const unsigned e0 = (g == 2) ? a0[0] : 0u, e1v = (g == 2) ? a1[0] : 0u;
                a0 = zz; a1 = zz;
                a0[h & 3] = e0; a1[h & 3] = e1v;
            }
#pragma unroll
            for (int s = 0; s < 4; ++s) {
                const f32x4 t0 = mfma16(asbf8(a0), asbf8(pq[s][h]), z4);
                const f32x4 t1 = mfma16(asbf8(a1), asbf8(pq[s][h]), z4);
                const float rd = 1.0f / (t1[3] + 1e-6f);
                af[s][h >> 1][(h & 1) * 2]     = pk2(t0[0] * rd, t0[1] * rd);
                af[s][h >> 1][(h & 1) * 2 + 1] = pk2(t0[2] * rd, t0[3] * rd);
                ax[s][h >> 2] |= pk2(t1[0] * rd, t1[1] * rd);
            }
        }
    }
    f32x4 acc[4][5];
#pragma unroll
    for (int s = 0; s < 4; ++s)
#pragma unroll
        for (int t = 0; t < 5; ++t) acc[s][t] = z4;
#pragma unroll
    for (int ks = 0; ks < 4; ++ks)
#pragma unroll
        for (int t = 0; t < 5; ++t)
#pragma unroll
            for (int s = 0; s < 4; ++s) acc[s][t] = mfma16(asbf8(wf[ks][t]), asbf8(af[s][ks]), acc[s][t]);
#pragma unroll
    for (int t = 0; t < 5; ++t)
#pragma unroll
        for (int s = 0; s < 4; ++s) acc[s][t] = mfma16k16(asbf4(wx[t]), asbf4(ax[s]), acc[s][t]);

#pragma unroll
    for (int s = 0; s < 4; ++s)
#pragma unroll
        for (int t = 0; t < 5; ++t) lred[((w * 4 + s) * 5 + t) * 64 + lane] = acc[s][t];
    __syncthreads();
    f32x4 r[5];
#pragma unroll
    for (int t = 0; t < 5; ++t)
        r[t] = (lred[((0 * 4 + w) * 5 + t) * 64 + lane] + lred[((1 * 4 + w) * 5 + t) * 64 + lane]) +
               (lred[((2 * 4 + w) * 5 + t) * 64 + lane] + lred[((3 * 4 + w) * 5 + t) * 64 + lane]);
    u32x4 y0, y1; u32x2 y2;
    y0[0] = pk2(bflo(xo0[0]) + r[0][0], bfhi(xo0[0]) + r[0][1]);
    y0[1] = pk2(bflo(xo0[1]) + r[0][2], bfhi(xo0[1]) + r[0][3]);
    y0[2] = pk2(bflo(xo0[2]) + r[1][0], bfhi(xo0[2]) + r[1][1]);
    y0[3] = pk2(bflo(xo0[3]) + r[1][2], bfhi(xo0[3]) + r[1][3]);
    y1[0] = pk2(bflo(xo1[0]) + r[2][0], bfhi(xo1[0]) + r[2][1]);
    y1[1] = pk2(bflo(xo1[1]) + r[2][2], bfhi(xo1[1]) + r[2][3]);
    y1[2] = pk2(bflo(xo1[2]) + r[3][0], bfhi(xo1[2]) + r[3][1]);
    y1[3] = pk2(bflo(xo1[3]) + r[3][2], bfhi(xo1[3]) + r[3][3]);
    y2[0] = pk2(bflo(xo2[0]) + r[4][0], bfhi(xo2[0]) + r[4][1]);
    y2[1] = pk2(bflo(xo2[1]) + r[4][2], bfhi(xo2[1]) + r[4][3]);
    if constexpr (FINAL) {
        uint16_t* orow = out + (size_t)(stw * 16 + r16) * D_ + nb * 72;
        *(u32x4*)(orow + g * 8) = y0;
        *(u32x4*)(orow + 32 + g * 8) = y1;
        if (g < 2) *(u32x2*)(orow + 64 + g * 4) = y2;
    } else {
        Xnew[i0] = y0; Xnew[i1] = y1;
        if (g < 2) ((u32x2*)(Xnew + i2))[g] = y2;
        float s = 0.f;
#pragma unroll
        for (int j = 0; j < 4; ++j) s += (bflo(y0[j]) + bfhi(y0[j])) + (bflo(y1[j]) + bfhi(y1[j]));
        if (g < 2) s += (bflo(y2[0]) + bfhi(y2[0])) + (bflo(y2[1]) + bfhi(y2[1]));
        s += __shfl_xor(s, 16); s += __shfl_xor(s, 32);
        const float mu = s * (1.0f / 72.0f);
        float m2 = 0.f;
#pragma unroll
        for (int j = 0; j < 4; ++j) {
            float a = bflo(y0[j]) - mu, b = bfhi(y0[j]) - mu, c = bflo(y1[j]) - mu, d = bfhi(y1[j]) - mu;
            m2 += (a * a + b * b) + (c * c + d * d);
        }
        if (g < 2) {
            float a = bflo(y2[0]) - mu, b = bfhi(y2[0]) - mu, c = bflo(y2[1]) - mu, d = bfhi(y2[1]) - mu;
            m2 += (a * a + b * b) + (c * c + d * d);
        }
        m2 += __shfl_xor(m2, 16); m2 += __shfl_xor(m2, 32);
        if (g == 0) stats[nb * S_ + stw * 16 + r16] = (f32x2){s, m2};
    }
}

DI void glds16(const void* gp, unsigned lds_base) {
    unsigned sv;
    asm volatile("s_mov_b32 %0, m0\n\ts_mov_b32 m0, %2\n\ts_nop 0\n\tglobal_load_lds_dwordx4 %1, off\n\ts_mov_b32 m0, %0"
                 : "=&s"(sv) : "v"(gp), "s"(lds_base) : "memory");
}
DI float max16(const f32x16& s) {
    float a = fmaxf(fmaxf(s[0], s[1]), s[2]), b = fmaxf(fmaxf(s[3], s[4]), s[5]);
    a = fmaxf(fmaxf(a, s[6]), s[7]); b = fmaxf(fmaxf(b, s[8]), s[9]);
    a = fmaxf(fmaxf(a, s[10]), s[11]); b = fmaxf(fmaxf(b, s[12]), s[13]);
    a = fmaxf(fmaxf(a, s[14]), s[15]);
    return fmaxf(a, b);
}
DI float xhalf_max(float m) {
    auto rr = __builtin_amdgcn_permlane32_swap(__float_as_uint(m), __float_as_uint(m), false, false);
    return fmaxf(__uint_as_float(rr[0]), __uint_as_float(rr[1]));
}
DI f32x16 splat16(float v) { f32x16 r;
#pragma unroll
    for (int i = 0; i < 16; ++i) r[i] = v;
    return r; }

__global__ __launch_bounds__(512, 2) void attn_kernel(const char* __restrict__ QM, const char* __restrict__ QX,
                                                      const char* __restrict__ IMG, char* __restrict__ OM,
                                                      char* __restrict__ OX) {
    __shared__ __attribute__((aligned(16))) char lds[IMG_HEAD + 8192];
    typedef __attribute__((address_space(3))) const u32x4* lptr;
    const int tid = threadIdx.x, lane = tid & 63, w = __builtin_amdgcn_readfirstlane(tid >> 6);
    const int r32 = lane & 31, hh = lane >> 5;
    int head, qb;
    { const int b = blockIdx.x, xcd = b & 7, i = b >> 3; head = xcd * 4 + (i >> 3); qb = i & 7; }
    const int qg = w & 3, kh = w >> 2;
    const int qt0 = qb * 8 + qg * 2;

    u32x4 qm[2]; unsigned qx[2];
    {
        const char* p0 = QM + (((size_t)head * 64 + qt0) * 64 + lane) * 16;
        const char* p1 = QX + ((size_t)head * S_ + qt0 * 32 + r32) * 4;
        asm volatile("global_load_dwordx4 %0, %4, off\n\tglobal_load_dwordx4 %1, %4, off offset:1024\n\t"
                     "global_load_dword %2, %5, off\n\tglobal_load_dword %3, %5, off offset:128"
                     : "=&v"(qm[0]), "=&v"(qm[1]), "=&v"(qx[0]), "=&v"(qx[1]) : "v"(p0), "v"(p1) : "memory");
    }
    {
        const char* src = IMG + (size_t)head * IMG_HEAD + lane * 16;
        const unsigned lbase = (unsigned)(uintptr_t)lds;
#pragma unroll
        for (int i = 0; i < 19; ++i) {
            int idx = i * 8 + w;
            if (idx >= 148) idx -= 8;
            int piece;
            if (i < 3) piece = (idx < 12) ? idx : 62 + idx;
            else { const int j = idx - 24; piece = (j & 1) ? 86 + (j >> 1) : 12 + (j >> 1); }
            glds16(src + piece * 1024, lbase + piece * 1024);
        }
    }
    asm volatile("s_waitcnt vmcnt(16)\n\ts_barrier" : "+v"(qm[0]), "+v"(qm[1]), "+v"(qx[0]), "+v"(qx[1]) :: "memory");

    bf16x8 qf[2]; bf16x4 qx0[2], qx1[2];
#pragma unroll
    for (int t = 0; t < 2; ++t) {
        qf[t] = asbf8(qm[t]);
        const unsigned e = hh ? 0u : qx[t];
        qx0[t] = asbf4((u32x2){e, 0u});
        qx1[t] = asbf4((u32x2){0u, e});
    }
    const char* gbase = lds + (size_t)kh * 8 * IMG_GROUP;
    const int offK = lane * 16, offX = 4096 + r32 * 16, offV = 4608 + hh * 304 + vpos(r32) * 16;
    float* xch = (float*)(lds + IMG_HEAD);

    f32x16 negm[2], o[2];
    float mrow[2];
    {
        const u32x4 kx = *(lptr)(gbase + offX);
        const bf16x8 km = asbf8(*(lptr)(gbase + offK));
        const f32x16 z = splat16(0.f);
#pragma unroll
        for (int t = 0; t < 2; ++t) {
            f32x16 s = mfma32k8(asbf4((u32x2){kx[0], kx[1]}), qx0[t], z);
            s = mfma32(km, qf[t], s);
            mrow[t] = xhalf_max(max16(s));
        }
    }
    bool all_loaded = false;
    for (int attempt = 0; attempt < 2; ++attempt) {
#pragma unroll
        for (int t = 0; t < 2; ++t) { negm[t] = splat16(-mrow[t]); o[t] = splat16(0.f); }
        for (int grp = 0; grp < 8; ++grp) {
            const char* gb = gbase + grp * IMG_GROUP;
            const u32x4 kx = *(lptr)(gb + offX);
#pragma unroll
            for (int t4 = 0; t4 < 4; ++t4) {
                const bf16x8 km = asbf8(*(lptr)(gb + t4 * 1024 + offK));
                const bf16x8 v0 = asbf8(*(lptr)(gb + t4 * 1216 + offV));
                const bf16x8 v1 = asbf8(*(lptr)(gb + t4 * 1216 + 608 + offV));
                const bf16x4 kxp = asbf4((u32x2){kx[t4 & 2], kx[(t4 & 2) + 1]});
#pragma unroll
                for (int t = 0; t < 2; ++t) {
                    f32x16 s = mfma32k8(kxp, (t4 & 1) ? qx1[t] : qx0[t], negm[t]);
                    s = mfma32(km, qf[t], s);
                    u32x4 p0, p1;
#pragma unroll
                    for (int j = 0; j < 4; ++j) {
                        p0[j] = pk2(__builtin_amdgcn_exp2f(s[2 * j]), __builtin_amdgcn_exp2f(s[2 * j + 1]));
                        p1[j] = pk2(__builtin_amdgcn_exp2f(s[8 + 2 * j]), __builtin_amdgcn_exp2f(s[8 + 2 * j + 1]));
                    }
                    o[t] = mfma32(v0, asbf8(p0), o[t]);
                    o[t] = mfma32(v1, asbf8(p1), o[t]);
                }
            }
            if (!all_loaded) {
                asm volatile("s_waitcnt vmcnt(0)\n\ts_barrier" ::: "memory");
                all_loaded = true;
            }
        }
        const bool bad = !(o[0][10] < 1.0995e12f) || !(o[1][10] < 1.0995e12f);
        const int anybad = __syncthreads_or(bad ? 1 : 0);
        if (!anybad || attempt == 1) break;
        float mx[2] = {-INFINITY, -INFINITY};
        const f32x16 z = splat16(0.f);
        for (int grp = 0; grp < 8; ++grp) {
            const char* gb = gbase + grp * IMG_GROUP;
            const u32x4 kx = *(lptr)(gb + offX);
#pragma unroll
            for (int t4 = 0; t4 < 4; ++t4) {
                const bf16x8 km = asbf8(*(lptr)(gb + t4 * 1024 + offK));
                const bf16x4 kxp = asbf4((u32x2){kx[t4 & 2], kx[(t4 & 2) + 1]});
#pragma unroll
                for (int t = 0; t < 2; ++t) {
                    f32x16 s = mfma32k8(kxp, (t4 & 1) ? qx1[t] : qx0[t], z);
                    s = mfma32(km, qf[t], s);
                    mx[t] = fmaxf(mx[t], max16(s));
                }
            }
        }
        mrow[0] = xhalf_max(mx[0]); mrow[1] = xhalf_max(mx[1]);
    }
    {
        float* dst = (float*)lds + (size_t)(w * 12) * 64 + lane;
        const int ts = 1 - kh;
#pragma unroll
        for (int i = 0; i < 11; ++i) dst[i * 64] = kh ? o[0][i] : o[1][i];
        dst[11 * 64] = kh ? mrow[0] : mrow[1];
        (void)ts; (void)xch;
    }
    __syncthreads();
    {
        const float* src = (const float*)lds + (size_t)((w ^ 4) * 12) * 64 + lane;
        const float mo = kh ? mrow[1] : mrow[0];
        const float mp = src[11 * 64];
        const float mm = fmaxf(mo, mp);
        const float fo = __builtin_amdgcn_exp2f(mo - mm), fp = __builtin_amdgcn_exp2f(mp - mm);
        float v[11];
#pragma unroll
        for (int i = 0; i < 11; ++i) v[i] = (kh ? o[1][i] : o[0][i]) * fo + src[i * 64] * fp;
        const float rl = 1.0f / v[10];
        const int q = (qt0 + kh) * 32 + r32, st = q >> 4;
        const u32x4 m8 = {pk2(v[0] * rl, v[1] * rl), pk2(v[2] * rl, v[3] * rl), pk2(v[4] * rl, v[5] * rl), pk2(v[6] * rl, v[7] * rl)};
        *(u32x4*)(OM + ((size_t)st * 16 + (head >> 1)) * 1024 + ((2 * (head & 1) + hh) * 16 + (q & 15)) * 16) = m8;
        if (hh == 0)
            *(unsigned*)(OX + ((size_t)st * 4 + (head >> 3)) * 512 + ((head & 3) * 16 + (q & 15)) * 8 + ((head >> 2) & 1) * 4) =
                pk2(v[8] * rl, v[9] * rl);
    }
}

extern "C" void kernel_launch(void* const* d_in, const int* in_sizes, int n_in,
                              void* d_out, int out_size, void* d_ws, size_t ws_size,
                              hipStream_t stream) {
    const uint16_t* x0  = (const uint16_t*)d_in[0];
    const uint16_t* wA  = (const uint16_t*)d_in[1];
    const uint16_t* woA = (const uint16_t*)d_in[2];
    const uint16_t* wB  = (const uint16_t*)d_in[4];
    const uint16_t* woB = (const uint16_t*)d_in[5];
    char* ws = (char*)d_ws;
    auto XT = [&](int i) { return (u32x4*)(ws + OFF_XT + (size_t)i * AL(XT_BYTES)); };
    auto ST = [&](int i) { return (f32x2*)(ws + OFF_ST + (size_t)i * AL(ST_BYTES)); };
    auto KV = [&](int i) { return (float*)(ws + OFF_KVB + (size_t)i * AL(KVB_BYTES)); };

    prep_kernel<<<PREP_W_BLOCKS + PREP_WO_BLOCKS + PREP_X_BLOCKS + PREP_Z_BLOCKS, 256, 0, stream>>>(x0, wA, woA, wB, woB, ws);
    for (int l = 0; l < 2; ++l) {
        qkv_kernel<true><<<256, 256, 0, stream>>>(XT(2 * l), ST(2 * l), (const u32x4*)(ws + OFF_WTA), (const float*)(ws + OFF_CSA),
                                                  ws + OFF_QM, ws + OFF_QX, ws + OFF_IMG, KV(l));
        oproj_kernel<true, false><<<256, 256, 0, stream>>>(ws + OFF_QM, ws + OFF_QX, KV(l), ws + OFF_WOA, XT(2 * l), XT(2 * l + 1),
                                                           ST(2 * l + 1), (uint16_t*)d_out);
        qkv_kernel<false><<<256, 256, 0, stream>>>(XT(2 * l + 1), ST(2 * l + 1), (const u32x4*)(ws + OFF_WTB), (const float*)(ws + OFF_CSB),
                                                   ws + OFF_QM, ws + OFF_QX, ws + OFF_IMG, KV(l));
        attn_kernel<<<256, 512, 0, stream>>>(ws + OFF_QM, ws + OFF_QX, ws + OFF_IMG, ws + OFF_OM, ws + OFF_OX);
        if (l == 0)
            oproj_kernel<false, false><<<256, 256, 0, stream>>>(ws + OFF_OM, ws + OFF_OX, KV(l), ws + OFF_WOB, XT(1), XT(2),
                                                                ST(2), (uint16_t*)d_out);
        else
            oproj_kernel<false, true><<<256, 256, 0, stream>>>(ws + OFF_OM, ws + OFF_OX, KV(l), ws + OFF_WOB, XT(3), XT(3),
                                                               ST(3), (uint16_t*)d_out);
    }
}
```

```cpp
#include <hip/hip_runtime.h>
#include <hip/hip_bf16.h>
#include <stdint.h>

typedef __attribute__((ext_vector_type(8))) short bf16x8;
typedef __attribute__((ext_vector_type(4))) short bf16x4;
typedef __attribute__((ext_vector_type(4))) float f32x4;
typedef __attribute__((ext_vector_type(2))) float f32x2;
typedef __attribute__((ext_vector_type(16))) float f32x16;
typedef __attribute__((ext_vector_type(4))) unsigned u32x4;
typedef __attribute__((ext_vector_type(2))) unsigned u32x2;
typedef __attribute__((ext_vector_type(2))) __bf16 bf16x2_t;

#define DI __device__ __forceinline__

constexpr int S_ = 2048, D_ = 576, N3 = 1728;

constexpr size_t WT_NT      = 136 * 1024;
constexpr size_t WT_BYTES   = 16 * WT_NT;
constexpr size_t WO_NB      = 82944;
constexpr size_t WO_BYTES   = 8 * WO_NB;
constexpr size_t XT_BYTES   = (size_t)S_ * D_ * 2;
constexpr size_t ST_BYTES   = 8 * S_ * 8;
constexpr size_t KVB_FLOATS = 32 * 19 * 20 + 128;
constexpr size_t KVB_BYTES  = KVB_FLOATS * 4;
constexpr int VT_CHUNK = 320, VT_TILE = 1280, VT_ONES = 4, VT_ZERO = 5;
constexpr size_t IMG_GROUP  = 9728;
constexpr size_t IMG_HEAD   = 16 * IMG_GROUP;
constexpr size_t AL(size_t x) { return (x + 4095) & ~(size_t)4095; }
constexpr size_t OFF_WTA = 0;
constexpr size_t OFF_WTB = OFF_WTA + AL(WT_BYTES);
constexpr size_t OFF_WOA = OFF_WTB + AL(WT_BYTES);
constexpr size_t OFF_WOB = OFF_WOA + AL(WO_BYTES);
constexpr size_t OFF_XT  = OFF_WOB + AL(WO_BYTES);
constexpr size_t OFF_ST  = OFF_XT + 4 * AL(XT_BYTES);
constexpr size_t OFF_KVB = OFF_ST + 4 * AL(ST_BYTES);
constexpr size_t CNT_BYTES = 2 * 96 * 256;
constexpr size_t OFF_CNT = OFF_KVB + 2 * AL(KVB_BYTES);
constexpr size_t L_PM  = 0;
constexpr int PX_ST = 2064;
constexpr size_t L_PX  = L_PM + AL((size_t)S_ * 32 * 32);
constexpr size_t L_QM  = L_PX + AL((size_t)128 * PX_ST);
constexpr size_t L_QX  = L_QM + AL((size_t)S_ * 32 * 32);
constexpr size_t L_IMG = L_QX + AL((size_t)S_ * 32 * 4);
constexpr size_t L_OM  = L_IMG + AL(32 * IMG_HEAD);
constexpr size_t L_OX  = L_OM + AL((size_t)S_ * 32 * 32);
constexpr size_t L_BYTES = L_OX + AL((size_t)S_ * 32 * 4);
constexpr size_t OFF_L = OFF_CNT + AL(CNT_BYTES);
constexpr int C_AG = 0, C_AT = 16, C_X1 = 17, C_KV = 33, C_O = 49, C_X2 = 57;
constexpr int LDS_BYTES = 163840, LDS_FLAG = LDS_BYTES - 64;

DI unsigned pk2(float a, float b) {
    f32x2 v = {a, b};
    bf16x2_t r = __builtin_convertvector(v, bf16x2_t);
    return __builtin_bit_cast(unsigned, r);
}
DI float bflo(unsigned u) { return __uint_as_float(u << 16); }
DI float bfhi(unsigned u) { return __uint_as_float(u & 0xffff0000u); }
DI f32x4 mfma16(bf16x8 a, bf16x8 b, f32x4 c) { return __builtin_amdgcn_mfma_f32_16x16x32_bf16(a, b, c, 0, 0, 0); }
DI f32x4 mfma16k16(bf16x4 a, bf16x4 b, f32x4 c) { return __builtin_amdgcn_mfma_f32_16x16x16bf16_1k(a, b, c, 0, 0, 0); }
DI f32x16 mfma32(bf16x8 a, bf16x8 b, f32x16 c) { return __builtin_amdgcn_mfma_f32_32x32x16_bf16(a, b, c, 0, 0, 0); }
DI f32x16 mfma32k8(bf16x4 a, bf16x4 b, f32x16 c) { return __builtin_amdgcn_mfma_f32_32x32x8bf16_1k(a, b, c, 0, 0, 0); }
DI bf16x8 asbf8(u32x4 v) { return __builtin_bit_cast(bf16x8, v); }
DI bf16x4 asbf4(u32x2 v) { return __builtin_bit_cast(bf16x4, v); }
DI float phi_s(float acc, float r, float rl2) {
    const float e = __builtin_amdgcn_exp2f(fminf(acc * rl2, 0.f));
    return fmaxf(fmaf(acc, r, 1e-6f), 1e-6f) + e;
}
DI int vpos(int d) { return d < 18 ? ((d == 4 || d == 5) ? d + 14 : d) : ((d == 18 || d == 22) ? VT_ONES : VT_ZERO); }
DI int swap34(int i) { return (i & 7) | ((i & 8) << 1) | ((i & 16) >> 1); }
DI float sum_x16(float v) {
    auto r = __builtin_amdgcn_permlane16_swap(__float_as_uint(v), __float_as_uint(v), false, false);
    return __uint_as_float(r[0]) + __uint_as_float(r[1]);
}
DI float sum_x32(float v) {
    auto r = __builtin_amdgcn_permlane32_swap(__float_as_uint(v), __float_as_uint(v), false, false);
    return __uint_as_float(r[0]) + __uint_as_float(r[1]);
}
DI float swap_nb(float v) {
    return __int_as_float(__builtin_amdgcn_update_dpp(0, __float_as_int(v), 0xB1, 0xF, 0xF, true));
}

DI void st16(void* p, u32x4 v) { asm volatile("global_store_dwordx4 %0, %1, off sc1\n\ts_nop 1" :: "v"(p), "v"(v) : "memory"); }
DI void st8(void* p, u32x2 v) { asm volatile("global_store_dwordx2 %0, %1, off sc1" :: "v"(p), "v"(v) : "memory"); }
DI void st4(void* p, unsigned v) { asm volatile("global_store_dword %0, %1, off sc1" :: "v"(p), "v"(v) : "memory"); }
#define GSLEEP 2
#define LSLEEP 1
typedef volatile __attribute__((address_space(3))) unsigned* ldsflag_t;
DI void wait_dep(unsigned* cnt, unsigned target, char* lds, unsigned id, int w, int sub = 0) {
    ldsflag_t flag = (ldsflag_t)(lds + LDS_FLAG);
    unsigned sp = 0;
    if (w == 0) {
        while (__any(__hip_atomic_load(cnt + sub * 64, __ATOMIC_RELAXED, __HIP_MEMORY_SCOPE_AGENT) < target)) {
            __builtin_amdgcn_s_sleep(GSLEEP);
            if (++sp > (1u << 16)) break;
        }
        *flag = id;
    } else {
        while (*flag < id) {
            __builtin_amdgcn_s_sleep(LSLEEP);
            if (++sp > (1u << 20)) break;
        }
    }
    asm volatile("" ::: "memory");
}
DI void drain_and_sync() { asm volatile("s_waitcnt vmcnt(0)" ::: "memory"); __syncthreads(); }
DI unsigned cnt_add(unsigned* p) { return __hip_atomic_fetch_add(p, 1u, __ATOMIC_RELAXED, __HIP_MEMORY_SCOPE_AGENT); }

DI void dma_loop(const char* src, unsigned dst, unsigned n, unsigned stride, unsigned l16) {
    asm volatile("s_mov_b32 s42, m0\n\ts_mov_b32 m0, %[dst]\n\ts_mov_b32 s43, %[n]\n\t"
                 "7:\n\t"
                 "global_load_lds_dwordx4 %[l16], s[40:41]\n\t"
                 "s_add_u32 m0, m0, %[st]\n\ts_add_u32 s40, s40, %[st]\n\ts_addc_u32 s41, s41, 0\n\t"
                 "s_sub_u32 s43, s43, 1\n\ts_cmp_lg_u32 s43, 0\n\ts_cbranch_scc1 7b\n\t"
                 "s_mov_b32 m0, s42"
                 : "+{s[40:41]}"(src) : [dst] "s"(dst), [n] "s"(n), [st] "s"(stride), [l16] "v"(l16) : "memory", "scc", "s42", "s43");
}

DI int qkv_col(bool isA, int nt, int ct, int r) {
    const int ha = 2 * nt;
    if (ct < 2 || (!isA && ct < 4)) {
        const int gp = r >> 2, i = r & 3;
        const int base = (ct < 2) ? 0 : 576;
        return base + (ha + (gp >> 1)) * 18 + 8 * (gp & 1) + 4 * (ct & 1) + i;
    }
    if (ct < 4) return 576 + (ha + (ct & 1)) * 18 + r;
    if (ct < 6) return 1152 + (ha + (ct & 1)) * 18 + r;
    if (r >= 12) return -1;
    return (r >> 2) * 576 + (ha + ((r >> 1) & 1)) * 18 + 16 + (r & 1);
}
DI size_t wt_frag_off(int nt, int f) { return (size_t)nt * WT_NT + (size_t)f * 1024; }

constexpr int PREP_W_BLOCKS = 192, PREP_WO_BLOCKS = 64;
__global__ __launch_bounds__(832) void prep_kernel(const uint16_t* __restrict__ x0, const uint16_t* __restrict__ wA,
                                                   const uint16_t* __restrict__ woA, const uint16_t* __restrict__ wB,
                                                   const uint16_t* __restrict__ woB, char* __restrict__ ws) {
    __shared__ float red[12][112];
    const int tid = threadIdx.x, lane = tid & 63, w = tid >> 6;
    int b = blockIdx.x;
    if (w == 12) {
        const int idx = b * 64 + lane, row = idx >> 3, part = idx & 7;
        const u32x4* src = (const u32x4*)(x0 + (size_t)row * D_ + part * 72);
        u32x4* XT = (u32x4*)(ws + OFF_XT);
        u32x4 v[9];
#pragma unroll
        for (int c = 0; c < 9; ++c) v[c] = src[c];
        float s = 0.f;
#pragma unroll
        for (int c = 0; c < 9; ++c)
#pragma unroll
            for (int j = 0; j < 4; ++j) s += bflo(v[c][j]) + bfhi(v[c][j]);
        const float mu = s * (1.0f / 72.0f);
        float m2 = 0.f;
#pragma unroll
        for (int c = 0; c < 9; ++c)
#pragma unroll
            for (int j = 0; j < 4; ++j) { float a = bflo(v[c][j]) - mu, bb = bfhi(v[c][j]) - mu; m2 += a * a + bb * bb; }
#pragma unroll
        for (int c = 0; c < 9; ++c) {
            const int cidx = part * 9 + c;
            XT[((size_t)(row >> 4) * 18 + (cidx >> 2)) * 64 + (cidx & 3) * 16 + (row & 15)] = v[c];
        }
        ((f32x2*)(ws + OFF_ST))[idx] = (f32x2){s, m2};
        const u32x4 zz = {0, 0, 0, 0};
        if (idx < (int)(KVB_BYTES / 16)) { ((u32x4*)(ws + OFF_KVB))[idx] = zz; ((u32x4*)(ws + OFF_KVB + AL(KVB_BYTES)))[idx] = zz; }
        if (idx < (int)(CNT_BYTES / 16)) ((u32x4*)(ws + OFF_CNT))[idx] = zz;
        if (idx < 256) *(u32x4*)(ws + OFF_L + (size_t)(idx >> 7) * L_BYTES + L_PX + (size_t)(idx & 127) * PX_ST + 2048) = zz;
        if (b < PREP_W_BLOCKS) __syncthreads();
        return;
    }
    if (b < PREP_W_BLOCKS) {
        const int wsel = b / 96, rem = b % 96, nt = rem / 6, kg = rem % 6;
        const uint16_t* W = wsel ? wB : wA;
        char* WT = ws + (wsel ? OFF_WTB : OFF_WTA);
        float s0 = 0.f, s1 = 0.f;
        const int ct = lane >> 3, r = (lane & 7) * 2;
        if (lane < 56) {
            const int col = qkv_col(wsel == 0, nt, ct, r);
            {
                const int ko = kg * 12 + w;
                unsigned v[8];
#pragma unroll
                for (int j = 0; j < 8; ++j) {
                    const unsigned t = *(const unsigned*)(W + (size_t)(ko * 8 + j) * N3 + (col >= 0 ? col : 0));
                    v[j] = col >= 0 ? t : 0u;
                }
                u32x4 lo, hi;
#pragma unroll
                for (int j = 0; j < 4; ++j) {
                    lo[j] = (v[2 * j] & 0xffffu) | (v[2 * j + 1] << 16);
                    hi[j] = (v[2 * j] >> 16) | (v[2 * j + 1] & 0xffff0000u);
                }
#pragma unroll
                for (int j = 0; j < 8; ++j) { s0 += bflo(v[j]); s1 += bfhi(v[j]); }
                const int ks = ko >> 2, g = ko & 3;
                u32x4* d = (u32x4*)(WT + wt_frag_off(nt, ks * 7 + ct)) + g * 16 + r;
                d[0] = lo; d[1] = hi;
            }
            red[w][ct * 16 + r] = s0; red[w][ct * 16 + r + 1] = s1;
        }
        __syncthreads();
        if (tid < 112) {
            const float cs = (((red[0][tid] + red[1][tid]) + (red[2][tid] + red[3][tid])) + ((red[4][tid] + red[5][tid]) + (red[6][tid] + red[7][tid]))) +
                             ((red[8][tid] + red[9][tid]) + (red[10][tid] + red[11][tid]));
            const unsigned hi = pk2(cs, 0.f) & 0xffffu;
            const unsigned lo = pk2(cs - bflo(hi), 0.f) & 0xffffu;
            uint16_t* d = (uint16_t*)(WT + wt_frag_off(nt, 126 + (tid >> 4))) + (tid & 15) * 8;
            d[kg] = (uint16_t)hi; d[128 + kg] = (uint16_t)lo; d[256 + kg] = (uint16_t)hi;
            if (kg == 0) {
                *(unsigned*)(d + 6) = 0u; *(unsigned*)(d + 128 + 6) = 0u; *(unsigned*)(d + 256 + 6) = 0u;
                *(u32x4*)(d + 384) = (u32x4){0, 0, 0, 0};
            }
        }
        return;
    }
    b -= PREP_W_BLOCKS;
    {
        const int wsel = b >> 5, nb = (b >> 2) & 7, wv = b & 3;
        const uint16_t* W = wsel ? woB : woA;
        char* dst = ws + (wsel ? OFF_WOB : OFF_WOA) + (size_t)nb * WO_NB;
        if (const int task = tid; task < 576 + 144) {
            const bool ex = task >= 576;
            const int tt = ex ? task - 576 : task;
            const int cp = tt % 36, kq = tt / 36;
            const int c = cp * 2, cc = c >> 3, pos = c & 7;
            int t, r;
            if (cc < 8) { t = 2 * (cc >> 2) + (pos >> 2); r = 4 * (cc & 3) + (pos & 3); } else { t = 4; r = pos; }
            const int col = nb * 72 + c;
            if (!ex) {
                const int ks = kq >> 2, g = kq & 3, P = 4 * wv + ks;
                unsigned v[8];
#pragma unroll
                for (int j = 0; j < 8; ++j) {
                    const int row = 36 * P + (wsel ? (g >> 1) * 18 + 4 * (g & 1) : 4 * g) + (j & 3) + (wsel ? 8 : 18) * (j >> 2);
                    v[j] = *(const unsigned*)(W + (size_t)row * D_ + col);
                }
                u32x4 lo, hi;
#pragma unroll
                for (int j = 0; j < 4; ++j) {
                    lo[j] = (v[2 * j] & 0xffffu) | (v[2 * j + 1] << 16);
                    hi[j] = (v[2 * j] >> 16) | (v[2 * j + 1] & 0xffff0000u);
                }
                const int i = P & 7;
                u32x4* d = (u32x4*)(dst + (2 * i + (i >= 4 ? 1 : 0) + (P >> 3)) * 4608 + t * 1024) + (t < 4 ? g * 16 : g * 8) + r;
                d[0] = lo; d[1] = hi;
            } else {
                const int g = kq;
                unsigned v[4];
#pragma unroll
                for (int j = 0; j < 4; ++j) {
                    const int row = (8 * wv + g + 4 * (j >> 1)) * 18 + 16 + (j & 1);
                    v[j] = *(const unsigned*)(W + (size_t)row * D_ + col);
                }
                u32x2 lo, hi;
#pragma unroll
                for (int j = 0; j < 2; ++j) {
                    lo[j] = (v[2 * j] & 0xffffu) | (v[2 * j + 1] << 16);
                    hi[j] = (v[2 * j] >> 16) | (v[2 * j + 1] & 0xffff0000u);
                }
                u32x2* d = (u32x2*)(dst + ((wv & 1) ? 78336 : 36864) + (wv >> 1) * 2304 + t * 512) + (t < 4 ? g * 16 : g * 8) + r;
                d[0] = lo; d[1] = hi;
            }
        }
        return;
    }
}

#define QKV_ASM_W \
    "s_mov_b32 s42, m0\n\t" \
    "s_mov_b32 m0, %[lb]\n\t" \
    "s_nop 0\n\t" \
    "global_load_lds_dwordx4 %[l16], s[40:41]\n\t" \
    "s_add_u32 m0, m0, 8192\n\t" \
    "s_add_u32 s40, s40, 8192\n\t" \
    "s_addc_u32 s41, s41, 0\n\t" \
    "global_load_lds_dwordx4 %[l16], s[40:41]\n\t" \
    "s_add_u32 m0, m0, 8192\n\t" \
    "s_add_u32 s40, s40, 8192\n\t" \
    "s_addc_u32 s41, s41, 0\n\t" \
    "global_load_lds_dwordx4 %[l16], s[40:41]\n\t" \
    "s_add_u32 m0, m0, 8192\n\t" \
    "s_add_u32 s40, s40, 8192\n\t" \
    "s_addc_u32 s41, s41, 0\n\t" \
    "global_load_lds_dwordx4 %[l16], s[40:41]\n\t" \
    "s_add_u32 m0, m0, 8192\n\t" \
    "s_add_u32 s40, s40, 8192\n\t" \
    "s_addc_u32 s41, s41, 0\n\t" \
    "global_load_lds_dwordx4 %[l16], s[40:41]\n\t" \
    "s_add_u32 m0, m0, 8192\n\t" \
    "s_add_u32 s40, s40, 8192\n\t" \
    "s_addc_u32 s41, s41, 0\n\t" \
    "global_load_lds_dwordx4 %[l16], s[40:41]\n\t" \
    "s_add_u32 m0, m0, 8192\n\t" \
    "s_add_u32 s40, s40, 8192\n\t" \
    "s_addc_u32 s41, s41, 0\n\t" \
    "global_load_lds_dwordx4 %[l16], s[40:41]\n\t" \
    "s_add_u32 m0, m0, 8192\n\t" \
    "s_add_u32 s40, s40, 8192\n\t" \
    "s_addc_u32 s41, s41, 0\n\t" \
    "global_load_lds_dwordx4 %[l16], s[40:41]\n\t" \
    "s_add_u32 m0, m0, 8192\n\t" \
    "s_add_u32 s40, s40, 8192\n\t" \
    "s_addc_u32 s41, s41, 0\n\t" \
    "global_load_lds_dwordx4 %[l16], s[40:41]\n\t" \
    "s_add_u32 m0, m0, 8192\n\t" \
    "s_add_u32 s40, s40, 8192\n\t" \
    "s_addc_u32 s41, s41, 0\n\t" \
    "global_load_lds_dwordx4 %[l16], s[40:41]\n\t" \
    "s_add_u32 m0, m0, 8192\n\t" \
    "s_add_u32 s40, s40, 8192\n\t" \
    "s_addc_u32 s41, s41, 0\n\t" \
    "global_load_lds_dwordx4 %[l16], s[40:41]\n\t" \
    "s_add_u32 m0, m0, 8192\n\t" \
    "s_add_u32 s40, s40, 8192\n\t" \
    "s_addc_u32 s41, s41, 0\n\t" \
    "global_load_lds_dwordx4 %[l16], s[40:41]\n\t" \
    "s_add_u32 m0, m0, 8192\n\t" \
    "s_add_u32 s40, s40, 8192\n\t" \
    "s_addc_u32 s41, s41, 0\n\t" \
    "global_load_lds_dwordx4 %[l16], s[40:41]\n\t" \
    "s_add_u32 m0, m0, 8192\n\t" \
    "s_add_u32 s40, s40, 8192\n\t" \
    "s_addc_u32 s41, s41, 0\n\t" \
    "global_load_lds_dwordx4 %[l16], s[40:41]\n\t" \
    "s_add_u32 m0, m0, 8192\n\t" \
    "s_add_u32 s40, s40, 8192\n\t" \
    "s_addc_u32 s41, s41, 0\n\t" \
    "global_load_lds_dwordx4 %[l16], s[40:41]\n\t" \
    "s_add_u32 m0, m0, 8192\n\t" \
    "s_add_u32 s40, s40, 8192\n\t" \
    "s_addc_u32 s41, s41, 0\n\t" \
    "global_load_lds_dwordx4 %[l16], s[40:41]\n\t" \
    "s_add_u32 m0, m0, 8192\n\t" \
    "s_add_u32 s40, s40, 8192\n\t" \
    "s_addc_u32 s41, s41, 0\n\t" \
    "global_load_lds_dwordx4 %[l16], s[40:41]\n\t" \
    "s_mov_b32 m0, s42\n\t"
#define QKV_ASM_SA \
    "global_load_dwordx4 v[36:39], v1, %[ss] offset:0 sc1\n\t" \
    "global_load_dwordx4 v[40:43], v1, %[ss] offset:1024 sc1\n\t" \
    "s_waitcnt vmcnt(1)\n\t" \
    "v_add_f32 v28, v36, v38\n\t" \
    "v_mov_b32 v29, v28\n\t" \
    "s_nop 1\n\t" \
    "v_permlane16_swap_b32 v28, v29\n\t" \
    "v_add_f32 v28, v28, v29\n\t" \
    "v_mov_b32 v29, v28\n\t" \
    "s_nop 1\n\t" \
    "v_permlane32_swap_b32 v28, v29\n\t" \
    "v_add_f32 v28, v28, v29\n\t" \
    "v_mul_f32 v18, 0xbae38e39, v28\n\t" \
    "v_fmamk_f32 v30, v36, 0x3c638e39, v18\n\t" \
    "v_fmamk_f32 v31, v38, 0x3c638e39, v18\n\t" \
    "v_mul_f32 v30, v30, v30\n\t" \
    "v_fmac_f32 v30, v31, v31\n\t" \
    "v_add_f32 v28, v37, v39\n\t" \
    "v_fmac_f32 v28, 0x42900000, v30\n\t" \
    "v_mov_b32 v29, v28\n\t" \
    "s_nop 1\n\t" \
    "v_permlane16_swap_b32 v28, v29\n\t" \
    "v_add_f32 v28, v28, v29\n\t" \
    "v_mov_b32 v29, v28\n\t" \
    "s_nop 1\n\t" \
    "v_permlane32_swap_b32 v28, v29\n\t" \
    "v_add_f32 v28, v28, v29\n\t" \
    "v_mov_b32 v29, 0x3727c5ac\n\t" \
    "v_fmac_f32 v29, 0x3ae38e39, v28\n\t" \
    "v_rsq_f32 v8, v29\n\t" \
    "s_nop 1\n\t" \
    "ds_write_b32 v5, v8 offset:0\n\t" \
    "ds_write_b32 v5, v18 offset:512\n\t" \
    "s_waitcnt vmcnt(0)\n\t" \
    "v_add_f32 v32, v40, v42\n\t" \
    "v_mov_b32 v33, v32\n\t" \
    "s_nop 1\n\t" \
    "v_permlane16_swap_b32 v32, v33\n\t" \
    "v_add_f32 v32, v32, v33\n\t" \
    "v_mov_b32 v33, v32\n\t" \
    "s_nop 1\n\t" \
    "v_permlane32_swap_b32 v32, v33\n\t" \
    "v_add_f32 v32, v32, v33\n\t" \
    "v_mul_f32 v19, 0xbae38e39, v32\n\t" \
    "v_fmamk_f32 v34, v40, 0x3c638e39, v19\n\t" \
    "v_fmamk_f32 v35, v42, 0x3c638e39, v19\n\t" \
    "v_mul_f32 v34, v34, v34\n\t" \
    "v_fmac_f32 v34, v35, v35\n\t" \
    "v_add_f32 v32, v41, v43\n\t" \
    "v_fmac_f32 v32, 0x42900000, v34\n\t" \
    "v_mov_b32 v33, v32\n\t" \
    "s_nop 1\n\t" \
    "v_permlane16_swap_b32 v32, v33\n\t" \
    "v_add_f32 v32, v32, v33\n\t" \
    "v_mov_b32 v33, v32\n\t" \
    "s_nop 1\n\t" \
    "v_permlane32_swap_b32 v32, v33\n\t" \
    "v_add_f32 v32, v32, v33\n\t" \
    "v_mov_b32 v33, 0x3727c5ac\n\t" \
    "v_fmac_f32 v33, 0x3ae38e39, v32\n\t" \
    "v_rsq_f32 v9, v33\n\t" \
    "s_nop 1\n\t" \
    "ds_write_b32 v5, v9 offset:64\n\t" \
    "ds_write_b32 v5, v19 offset:576\n\t" \
    "s_waitcnt lgkmcnt(0)\n\t" \
    "s_barrier\n\t" \
    "s_barrier\n\t" \
    "ds_read_b128 v[44:47], v7 offset:0\n\t" \
    "ds_read_b128 v[48:51], v7 offset:1024\n\t" \
    "ds_read_b128 v[72:75], v7 offset:2048\n\t" \
    "ds_read_b128 v[76:79], v7 offset:3072\n\t" \
    "s_waitcnt lgkmcnt(0)\n\t" \
    "s_nop 1\n\t"
#define QKV_ASM_SB \
    "global_load_dwordx4 v[36:39], v1, %[ss] offset:0 sc1\n\t" \
    "global_load_dwordx4 v[40:43], v1, %[ss] offset:1024 sc1\n\t" \
    "s_waitcnt vmcnt(1)\n\t" \
    "v_add_f32 v28, v36, v38\n\t" \
    "v_mov_b32 v29, v28\n\t" \
    "s_nop 1\n\t" \
    "v_permlane16_swap_b32 v28, v29\n\t" \
    "v_add_f32 v28, v28, v29\n\t" \
    "v_mov_b32 v29, v28\n\t" \
    "s_nop 1\n\t" \
    "v_permlane32_swap_b32 v28, v29\n\t" \
    "v_add_f32 v28, v28, v29\n\t" \
    "v_mul_f32 v18, 0xbae38e39, v28\n\t" \
    "v_fmamk_f32 v30, v36, 0x3c638e39, v18\n\t" \
    "v_fmamk_f32 v31, v38, 0x3c638e39, v18\n\t" \
    "v_mul_f32 v30, v30, v30\n\t" \
    "v_fmac_f32 v30, v31, v31\n\t" \
    "v_add_f32 v28, v37, v39\n\t" \
    "v_fmac_f32 v28, 0x42900000, v30\n\t" \
    "v_mov_b32 v29, v28\n\t" \
    "s_nop 1\n\t" \
    "v_permlane16_swap_b32 v28, v29\n\t" \
    "v_add_f32 v28, v28, v29\n\t" \
    "v_mov_b32 v29, v28\n\t" \
    "s_nop 1\n\t" \
    "v_permlane32_swap_b32 v28, v29\n\t" \
    "v_add_f32 v28, v28, v29\n\t" \
    "v_mov_b32 v29, 0x3727c5ac\n\t" \
    "v_fmac_f32 v29, 0x3ae38e39, v28\n\t" \
    "v_rsq_f32 v8, v29\n\t" \
    "s_nop 1\n\t" \
    "ds_write_b32 v5, v8 offset:0\n\t" \
    "ds_write_b32 v5, v18 offset:512\n\t" \
    "s_waitcnt vmcnt(0)\n\t" \
    "v_add_f32 v32, v40, v42\n\t" \
    "v_mov_b32 v33, v32\n\t" \
    "s_nop 1\n\t" \
    "v_permlane16_swap_b32 v32, v33\n\t" \
    "v_add_f32 v32, v32, v33\n\t" \
    "v_mov_b32 v33, v32\n\t" \
    "s_nop 1\n\t" \
    "v_permlane32_swap_b32 v32, v33\n\t" \
    "v_add_f32 v32, v32, v33\n\t" \
    "v_mul_f32 v19, 0xbae38e39, v32\n\t" \
    "v_fmamk_f32 v34, v40, 0x3c638e39, v19\n\t" \
    "v_fmamk_f32 v35, v42, 0x3c638e39, v19\n\t" \
    "v_mul_f32 v34, v34, v34\n\t" \
    "v_fmac_f32 v34, v35, v35\n\t" \
    "v_add_f32 v32, v41, v43\n\t" \
    "v_fmac_f32 v32, 0x42900000, v34\n\t" \
    "v_mov_b32 v33, v32\n\t" \
    "s_nop 1\n\t" \
    "v_permlane16_swap_b32 v32, v33\n\t" \
    "v_add_f32 v32, v32, v33\n\t" \
    "v_mov_b32 v33, v32\n\t" \
    "s_nop 1\n\t" \
    "v_permlane32_swap_b32 v32, v33\n\t" \
    "v_add_f32 v32, v32, v33\n\t" \
    "v_mov_b32 v33, 0x3727c5ac\n\t" \
    "v_fmac_f32 v33, 0x3ae38e39, v32\n\t" \
    "v_rsq_f32 v9, v33\n\t" \
    "s_nop 1\n\t" \
    "ds_write_b32 v5, v9 offset:64\n\t" \
    "ds_write_b32 v5, v19 offset:576\n\t" \
    "s_waitcnt lgkmcnt(0)\n\t" \
    "s_barrier\n\t" \
    "s_nop 1\n\t"
#define QKV_ASM_CA \
    "global_load_dwordx4 v[156:159], v0, %[sg] offset:-4096 sc1\n\t" \
    "global_load_dwordx4 v[160:163], v1, %[sg] offset:-4096 sc1\n\t" \
    "global_load_dwordx4 v[164:167], v0, %[sg] offset:-3072 sc1\n\t" \
    "global_load_dwordx4 v[168:171], v1, %[sg] offset:-3072 sc1\n\t" \
    "global_load_dwordx4 v[172:175], v0, %[sg] offset:-2048 sc1\n\t" \
    "global_load_dwordx4 v[176:179], v1, %[sg] offset:-2048 sc1\n\t" \
    "global_load_dwordx4 v[180:183], v0, %[sg] offset:-1024 sc1\n\t" \
    "global_load_dwordx4 v[184:187], v1, %[sg] offset:-1024 sc1\n\t" \
    "v_mov_b32 v237, 1\n\t" \
    "s_waitcnt vmcnt(8)\n\t" \
    "ds_add_u32 v236, v237\n\t" \
    "ds_read_b32 v237, v236 offset:0\n\t" \
    "s_waitcnt lgkmcnt(0)\n\t" \
    "22:\n\t" \
    "v_readfirstlane_b32 s46, v237\n\t" \
    "s_cmp_ge_u32 s46, %[wb]\n\t" \
    "s_cbranch_scc1 23f\n\t" \
    "s_sleep 1\n\t" \
    "ds_read_b32 v237, v236 offset:0\n\t" \
    "s_waitcnt lgkmcnt(0)\n\t" \
    "s_branch 22b\n\t" \
    "23:\n\t" \
    "ds_read_b128 v[100:103], v2 offset:0\n\t" \
    "ds_read_b128 v[104:107], v2 offset:1024\n\t" \
    "ds_read_b128 v[108:111], v2 offset:2048\n\t" \
    "ds_read_b128 v[112:115], v2 offset:3072\n\t" \
    "ds_read_b128 v[116:119], v2 offset:4096\n\t" \
    "ds_read_b128 v[120:123], v2 offset:5120\n\t" \
    "ds_read_b128 v[124:127], v2 offset:6144\n\t" \
    "s_waitcnt vmcnt(7)\n\t" \
    "s_waitcnt vmcnt(6)\n\t" \
    "s_waitcnt lgkmcnt(0)\n\t" \
    "v_mfma_f32_16x16x32_bf16 v[44:47], v[100:103], v[156:159], 0\n\t" \
    "ds_read_b128 v[128:131], v2 offset:7168\n\t" \
    "v_mfma_f32_16x16x32_bf16 v[72:75], v[100:103], v[160:163], 0\n\t" \
    "ds_read_b128 v[132:135], v2 offset:8192\n\t" \
    "v_mfma_f32_16x16x32_bf16 v[48:51], v[104:107], v[156:159], 0\n\t" \
    "ds_read_b128 v[136:139], v2 offset:9216\n\t" \
    "v_mfma_f32_16x16x32_bf16 v[76:79], v[104:107], v[160:163], 0\n\t" \
    "ds_read_b128 v[140:143], v2 offset:10240\n\t" \
    "v_mfma_f32_16x16x32_bf16 v[52:55], v[156:159], v[108:111], 0\n\t" \
    "ds_read_b128 v[144:147], v2 offset:11264\n\t" \
    "v_mfma_f32_16x16x32_bf16 v[80:83], v[160:163], v[108:111], 0\n\t" \
    "ds_read_b128 v[148:151], v2 offset:12288\n\t" \
    "v_mfma_f32_16x16x32_bf16 v[56:59], v[156:159], v[112:115], 0\n\t" \
    "ds_read_b128 v[152:155], v2 offset:13312\n\t" \
    "v_mfma_f32_16x16x32_bf16 v[84:87], v[160:163], v[112:115], 0\n\t" \
    "v_mfma_f32_16x16x32_bf16 v[60:63], v[156:159], v[116:119], 0\n\t" \
    "global_load_dwordx4 v[188:191], v0, %[sg] offset:0 sc1\n\t" \
    "global_load_dwordx4 v[192:195], v1, %[sg] offset:0 sc1\n\t" \
    "v_mfma_f32_16x16x32_bf16 v[88:91], v[160:163], v[116:119], 0\n\t" \
    "v_mfma_f32_16x16x32_bf16 v[64:67], v[156:159], v[120:123], 0\n\t" \
    "global_load_dwordx4 v[196:199], v0, %[sg] offset:1024 sc1\n\t" \
    "global_load_dwordx4 v[200:203], v1, %[sg] offset:1024 sc1\n\t" \
    "v_mfma_f32_16x16x32_bf16 v[92:95], v[160:163], v[120:123], 0\n\t" \
    "v_mfma_f32_16x16x32_bf16 v[68:71], v[156:159], v[124:127], 0\n\t" \
    "v_mfma_f32_16x16x32_bf16 v[96:99], v[160:163], v[124:127], 0\n\t" \
    "s_waitcnt vmcnt(9)\n\t" \
    "s_waitcnt vmcnt(8)\n\t" \
    "s_waitcnt lgkmcnt(0)\n\t" \
    "v_mfma_f32_16x16x32_bf16 v[44:47], v[128:131], v[164:167], v[44:47]\n\t" \
    "ds_read_b128 v[100:103], v2 offset:14336\n\t" \
    "v_mfma_f32_16x16x32_bf16 v[72:75], v[128:131], v[168:171], v[72:75]\n\t" \
    "ds_read_b128 v[104:107], v2 offset:15360\n\t" \
    "v_mfma_f32_16x16x32_bf16 v[48:51], v[132:135], v[164:167], v[48:51]\n\t" \
    "ds_read_b128 v[108:111], v2 offset:16384\n\t" \
    "v_mfma_f32_16x16x32_bf16 v[76:79], v[132:135], v[168:171], v[76:79]\n\t" \
    "ds_read_b128 v[112:115], v2 offset:17408\n\t" \
    "v_mfma_f32_16x16x32_bf16 v[52:55], v[164:167], v[136:139], v[52:55]\n\t" \
    "ds_read_b128 v[116:119], v2 offset:18432\n\t" \
    "v_mfma_f32_16x16x32_bf16 v[80:83], v[168:171], v[136:139], v[80:83]\n\t" \
    "ds_read_b128 v[120:123], v2 offset:19456\n\t" \
    "v_mfma_f32_16x16x32_bf16 v[56:59], v[164:167], v[140:143], v[56:59]\n\t" \
    "ds_read_b128 v[124:127], v2 offset:20480\n\t" \
    "v_mfma_f32_16x16x32_bf16 v[84:87], v[168:171], v[140:143], v[84:87]\n\t" \
    "v_mfma_f32_16x16x32_bf16 v[60:63], v[164:167], v[144:147], v[60:63]\n\t" \
    "global_load_dwordx4 v[204:207], v0, %[sg] offset:2048 sc1\n\t" \
    "global_load_dwordx4 v[208:211], v1, %[sg] offset:2048 sc1\n\t" \
    "v_mfma_f32_16x16x32_bf16 v[88:91], v[168:171], v[144:147], v[88:91]\n\t" \
    "v_mfma_f32_16x16x32_bf16 v[64:67], v[164:167], v[148:151], v[64:67]\n\t" \
    "global_load_dwordx4 v[212:215], v0, %[sg] offset:3072 sc1\n\t" \
    "global_load_dwordx4 v[216:219], v1, %[sg] offset:3072 sc1\n\t" \
    "v_mfma_f32_16x16x32_bf16 v[92:95], v[168:171], v[148:151], v[92:95]\n\t" \
    "v_mfma_f32_16x16x32_bf16 v[68:71], v[164:167], v[152:155], v[68:71]\n\t" \
    "v_mfma_f32_16x16x32_bf16 v[96:99], v[168:171], v[152:155], v[96:99]\n\t" \
    "s_waitcnt vmcnt(11)\n\t" \
    "s_waitcnt vmcnt(10)\n\t" \
    "s_waitcnt lgkmcnt(0)\n\t" \
    "v_mfma_f32_16x16x32_bf16 v[44:47], v[100:103], v[172:175], v[44:47]\n\t" \
    "ds_read_b128 v[128:131], v2 offset:21504\n\t" \
    "v_mfma_f32_16x16x32_bf16 v[72:75], v[100:103], v[176:179], v[72:75]\n\t" \
    "ds_read_b128 v[132:135], v2 offset:22528\n\t" \
    "v_mfma_f32_16x16x32_bf16 v[48:51], v[104:107], v[172:175], v[48:51]\n\t" \
    "ds_read_b128 v[136:139], v2 offset:23552\n\t" \
    "v_mfma_f32_16x16x32_bf16 v[76:79], v[104:107], v[176:179], v[76:79]\n\t" \
    "ds_read_b128 v[140:143], v2 offset:24576\n\t" \
    "v_mfma_f32_16x16x32_bf16 v[52:55], v[172:175], v[108:111], v[52:55]\n\t" \
    "ds_read_b128 v[144:147], v2 offset:25600\n\t" \
    "v_mfma_f32_16x16x32_bf16 v[80:83], v[176:179], v[108:111], v[80:83]\n\t" \
    "ds_read_b128 v[148:151], v2 offset:26624\n\t" \
    "v_mfma_f32_16x16x32_bf16 v[56:59], v[172:175], v[112:115], v[56:59]\n\t" \
    "ds_read_b128 v[152:155], v2 offset:27648\n\t" \
    "v_mfma_f32_16x16x32_bf16 v[84:87], v[176:179], v[112:115], v[84:87]\n\t" \
    "v_mfma_f32_16x16x32_bf16 v[60:63], v[172:175], v[116:119], v[60:63]\n\t" \
    "v_add_u32 v0, 8192, v0\n\t" \
    "global_load_dwordx4 v[156:159], v0, %[sg] offset:-4096 sc1\n\t" \
    "v_add_u32 v1, 8192, v1\n\t" \
    "global_load_dwordx4 v[160:163], v1, %[sg] offset:-4096 sc1\n\t" \
    "v_mfma_f32_16x16x32_bf16 v[88:91], v[176:179], v[116:119], v[88:91]\n\t" \
    "v_mfma_f32_16x16x32_bf16 v[64:67], v[172:175], v[120:123], v[64:67]\n\t" \
    "global_load_dwordx4 v[164:167], v0, %[sg] offset:-3072 sc1\n\t" \
    "global_load_dwordx4 v[168:171], v1, %[sg] offset:-3072 sc1\n\t" \
    "v_mfma_f32_16x16x32_bf16 v[92:95], v[176:179], v[120:123], v[92:95]\n\t" \
    "v_mfma_f32_16x16x32_bf16 v[68:71], v[172:175], v[124:127], v[68:71]\n\t" \
    "v_mfma_f32_16x16x32_bf16 v[96:99], v[176:179], v[124:127], v[96:99]\n\t" \
    "s_waitcnt vmcnt(13)\n\t" \
    "s_waitcnt vmcnt(12)\n\t" \
    "s_waitcnt lgkmcnt(0)\n\t" \
    "v_mfma_f32_16x16x32_bf16 v[44:47], v[128:131], v[180:183], v[44:47]\n\t" \
    "ds_read_b128 v[100:103], v2 offset:28672\n\t" \
    "v_mfma_f32_16x16x32_bf16 v[72:75], v[128:131], v[184:187], v[72:75]\n\t" \
    "ds_read_b128 v[104:107], v2 offset:29696\n\t" \
    "v_mfma_f32_16x16x32_bf16 v[48:51], v[132:135], v[180:183], v[48:51]\n\t" \
    "ds_read_b128 v[108:111], v2 offset:30720\n\t" \
    "v_mfma_f32_16x16x32_bf16 v[76:79], v[132:135], v[184:187], v[76:79]\n\t" \
    "ds_read_b128 v[112:115], v2 offset:31744\n\t" \
    "v_mfma_f32_16x16x32_bf16 v[52:55], v[180:183], v[136:139], v[52:55]\n\t" \
    "ds_read_b128 v[116:119], v2 offset:32768\n\t" \
    "v_mfma_f32_16x16x32_bf16 v[80:83], v[184:187], v[136:139], v[80:83]\n\t" \
    "ds_read_b128 v[120:123], v2 offset:33792\n\t" \
    "v_mfma_f32_16x16x32_bf16 v[56:59], v[180:183], v[140:143], v[56:59]\n\t" \
    "ds_read_b128 v[124:127], v2 offset:34816\n\t" \
    "v_mfma_f32_16x16x32_bf16 v[84:87], v[184:187], v[140:143], v[84:87]\n\t" \
    "v_mfma_f32_16x16x32_bf16 v[60:63], v[180:183], v[144:147], v[60:63]\n\t" \
    "global_load_dwordx4 v[172:175], v0, %[sg] offset:-2048 sc1\n\t" \
    "global_load_dwordx4 v[176:179], v1, %[sg] offset:-2048 sc1\n\t" \
    "v_mfma_f32_16x16x32_bf16 v[88:91], v[184:187], v[144:147], v[88:91]\n\t" \
    "v_mfma_f32_16x16x32_bf16 v[64:67], v[180:183], v[148:151], v[64:67]\n\t" \
    "v_mfma_f32_16x16x32_bf16 v[92:95], v[184:187], v[148:151], v[92:95]\n\t" \
    "v_mfma_f32_16x16x32_bf16 v[68:71], v[180:183], v[152:155], v[68:71]\n\t" \
    "v_mfma_f32_16x16x32_bf16 v[96:99], v[184:187], v[152:155], v[96:99]\n\t" \
    "s_waitcnt vmcnt(13)\n\t" \
    "s_waitcnt vmcnt(12)\n\t" \
    "s_waitcnt lgkmcnt(0)\n\t" \
    "v_mfma_f32_16x16x32_bf16 v[44:47], v[100:103], v[188:191], v[44:47]\n\t" \
    "ds_read_b128 v[128:131], v2 offset:35840\n\t" \
    "v_mfma_f32_16x16x32_bf16 v[72:75], v[100:103], v[192:195], v[72:75]\n\t" \
    "ds_read_b128 v[132:135], v2 offset:36864\n\t" \
    "v_mfma_f32_16x16x32_bf16 v[48:51], v[104:107], v[188:191], v[48:51]\n\t" \
    "ds_read_b128 v[136:139], v2 offset:37888\n\t" \
    "v_mfma_f32_16x16x32_bf16 v[76:79], v[104:107], v[192:195], v[76:79]\n\t" \
    "ds_read_b128 v[140:143], v2 offset:38912\n\t" \
    "v_mfma_f32_16x16x32_bf16 v[52:55], v[188:191], v[108:111], v[52:55]\n\t" \
    "ds_read_b128 v[144:147], v2 offset:39936\n\t" \
    "v_mfma_f32_16x16x32_bf16 v[80:83], v[192:195], v[108:111], v[80:83]\n\t" \
    "ds_read_b128 v[148:151], v2 offset:40960\n\t" \
    "v_mfma_f32_16x16x32_bf16 v[56:59], v[188:191], v[112:115], v[56:59]\n\t" \
    "ds_read_b128 v[152:155], v2 offset:41984\n\t" \
    "v_mfma_f32_16x16x32_bf16 v[84:87], v[192:195], v[112:115], v[84:87]\n\t" \
    "v_mfma_f32_16x16x32_bf16 v[60:63], v[188:191], v[116:119], v[60:63]\n\t" \
    "global_load_dwordx4 v[180:183], v0, %[sg] offset:-1024 sc1\n\t" \
    "global_load_dwordx4 v[184:187], v1, %[sg] offset:-1024 sc1\n\t" \
    "v_mfma_f32_16x16x32_bf16 v[88:91], v[192:195], v[116:119], v[88:91]\n\t" \
    "v_mfma_f32_16x16x32_bf16 v[64:67], v[188:191], v[120:123], v[64:67]\n\t" \
    "v_mfma_f32_16x16x32_bf16 v[92:95], v[192:195], v[120:123], v[92:95]\n\t" \
    "v_mfma_f32_16x16x32_bf16 v[68:71], v[188:191], v[124:127], v[68:71]\n\t" \
    "v_mfma_f32_16x16x32_bf16 v[96:99], v[192:195], v[124:127], v[96:99]\n\t" \
    "s_waitcnt vmcnt(13)\n\t" \
    "s_waitcnt vmcnt(12)\n\t" \
    "s_waitcnt lgkmcnt(0)\n\t" \
    "v_mfma_f32_16x16x32_bf16 v[44:47], v[128:131], v[196:199], v[44:47]\n\t" \
    "ds_read_b128 v[100:103], v2 offset:43008\n\t" \
    "v_mfma_f32_16x16x32_bf16 v[72:75], v[128:131], v[200:203], v[72:75]\n\t" \
    "ds_read_b128 v[104:107], v2 offset:44032\n\t" \
    "v_mfma_f32_16x16x32_bf16 v[48:51], v[132:135], v[196:199], v[48:51]\n\t" \
    "ds_read_b128 v[108:111], v2 offset:45056\n\t" \
    "v_mfma_f32_16x16x32_bf16 v[76:79], v[132:135], v[200:203], v[76:79]\n\t" \
    "ds_read_b128 v[112:115], v2 offset:46080\n\t" \
    "v_mfma_f32_16x16x32_bf16 v[52:55], v[196:199], v[136:139], v[52:55]\n\t" \
    "ds_read_b128 v[116:119], v2 offset:47104\n\t" \
    "v_mfma_f32_16x16x32_bf16 v[80:83], v[200:203], v[136:139], v[80:83]\n\t" \
    "ds_read_b128 v[120:123], v2 offset:48128\n\t" \
    "v_mfma_f32_16x16x32_bf16 v[56:59], v[196:199], v[140:143], v[56:59]\n\t" \
    "ds_read_b128 v[124:127], v2 offset:49152\n\t" \
    "v_mfma_f32_16x16x32_bf16 v[84:87], v[200:203], v[140:143], v[84:87]\n\t" \
    "v_mfma_f32_16x16x32_bf16 v[60:63], v[196:199], v[144:147], v[60:63]\n\t" \
    "global_load_dwordx4 v[188:191], v0, %[sg] offset:0 sc1\n\t" \
    "global_load_dwordx4 v[192:195], v1, %[sg] offset:0 sc1\n\t" \
    "v_mfma_f32_16x16x32_bf16 v[88:91], v[200:203], v[144:147], v[88:91]\n\t" \
    "v_mfma_f32_16x16x32_bf16 v[64:67], v[196:199], v[148:151], v[64:67]\n\t" \
    "v_mfma_f32_16x16x32_bf16 v[92:95], v[200:203], v[148:151], v[92:95]\n\t" \
    "v_mfma_f32_16x16x32_bf16 v[68:71], v[196:199], v[152:155], v[68:71]\n\t" \
    "v_mfma_f32_16x16x32_bf16 v[96:99], v[200:203], v[152:155], v[96:99]\n\t" \
    "s_waitcnt vmcnt(13)\n\t" \
    "s_waitcnt vmcnt(12)\n\t" \
    "s_waitcnt lgkmcnt(0)\n\t" \
    "v_mfma_f32_16x16x32_bf16 v[44:47], v[100:103], v[204:207], v[44:47]\n\t" \
    "ds_read_b128 v[128:131], v2 offset:50176\n\t" \
    "v_mfma_f32_16x16x32_bf16 v[72:75], v[100:103], v[208:211], v[72:75]\n\t" \
    "ds_read_b128 v[132:135], v2 offset:51200\n\t" \
    "v_mfma_f32_16x16x32_bf16 v[48:51], v[104:107], v[204:207], v[48:51]\n\t" \
    "ds_read_b128 v[136:139], v2 offset:52224\n\t" \
    "v_mfma_f32_16x16x32_bf16 v[76:79], v[104:107], v[208:211], v[76:79]\n\t" \
    "ds_read_b128 v[140:143], v2 offset:53248\n\t" \
    "v_mfma_f32_16x16x32_bf16 v[52:55], v[204:207], v[108:111], v[52:55]\n\t" \
    "ds_read_b128 v[144:147], v2 offset:54272\n\t" \
    "v_mfma_f32_16x16x32_bf16 v[80:83], v[208:211], v[108:111], v[80:83]\n\t" \
    "ds_read_b128 v[148:151], v2 offset:55296\n\t" \
    "v_mfma_f32_16x16x32_bf16 v[56:59], v[204:207], v[112:115], v[56:59]\n\t" \
    "ds_read_b128 v[152:155], v2 offset:56320\n\t" \
    "v_mfma_f32_16x16x32_bf16 v[84:87], v[208:211], v[112:115], v[84:87]\n\t" \
    "v_mfma_f32_16x16x32_bf16 v[60:63], v[204:207], v[116:119], v[60:63]\n\t" \
    "global_load_dwordx4 v[196:199], v0, %[sg] offset:1024 sc1\n\t" \
    "global_load_dwordx4 v[200:203], v1, %[sg] offset:1024 sc1\n\t" \
    "v_mfma_f32_16x16x32_bf16 v[88:91], v[208:211], v[116:119], v[88:91]\n\t" \
    "v_mfma_f32_16x16x32_bf16 v[64:67], v[204:207], v[120:123], v[64:67]\n\t" \
    "v_mfma_f32_16x16x32_bf16 v[92:95], v[208:211], v[120:123], v[92:95]\n\t" \
    "v_mfma_f32_16x16x32_bf16 v[68:71], v[204:207], v[124:127], v[68:71]\n\t" \
    "v_mfma_f32_16x16x32_bf16 v[96:99], v[208:211], v[124:127], v[96:99]\n\t" \
    "s_waitcnt vmcnt(13)\n\t" \
    "s_waitcnt vmcnt(12)\n\t" \
    "s_waitcnt lgkmcnt(0)\n\t" \
    "v_mfma_f32_16x16x32_bf16 v[44:47], v[128:131], v[212:215], v[44:47]\n\t" \
    "ds_read_b128 v[100:103], v2 offset:57344\n\t" \
    "v_mfma_f32_16x16x32_bf16 v[72:75], v[128:131], v[216:219], v[72:75]\n\t" \
    "ds_read_b128 v[104:107], v2 offset:58368\n\t" \
    "v_mfma_f32_16x16x32_bf16 v[48:51], v[132:135], v[212:215], v[48:51]\n\t" \
    "ds_read_b128 v[108:111], v2 offset:59392\n\t" \
    "v_mfma_f32_16x16x32_bf16 v[76:79], v[132:135], v[216:219], v[76:79]\n\t" \
    "ds_read_b128 v[112:115], v2 offset:60416\n\t" \
    "v_mfma_f32_16x16x32_bf16 v[52:55], v[212:215], v[136:139], v[52:55]\n\t" \
    "ds_read_b128 v[116:119], v2 offset:61440\n\t" \
    "v_mfma_f32_16x16x32_bf16 v[80:83], v[216:219], v[136:139], v[80:83]\n\t" \
    "ds_read_b128 v[120:123], v2 offset:62464\n\t" \
    "v_mfma_f32_16x16x32_bf16 v[56:59], v[212:215], v[140:143], v[56:59]\n\t" \
    "ds_read_b128 v[124:127], v2 offset:63488\n\t" \
    "v_mfma_f32_16x16x32_bf16 v[84:87], v[216:219], v[140:143], v[84:87]\n\t" \
    "v_mfma_f32_16x16x32_bf16 v[60:63], v[212:215], v[144:147], v[60:63]\n\t" \
    "global_load_dwordx4 v[204:207], v0, %[sg] offset:2048 sc1\n\t" \
    "global_load_dwordx4 v[208:211], v1, %[sg] offset:2048 sc1\n\t" \
    "v_mfma_f32_16x16x32_bf16 v[88:91], v[216:219], v[144:147], v[88:91]\n\t" \
    "v_mfma_f32_16x16x32_bf16 v[64:67], v[212:215], v[148:151], v[64:67]\n\t" \
    "v_mfma_f32_16x16x32_bf16 v[92:95], v[216:219], v[148:151], v[92:95]\n\t" \
    "v_mfma_f32_16x16x32_bf16 v[68:71], v[212:215], v[152:155], v[68:71]\n\t" \
    "v_mfma_f32_16x16x32_bf16 v[96:99], v[216:219], v[152:155], v[96:99]\n\t" \
    "s_waitcnt vmcnt(13)\n\t" \
    "s_waitcnt vmcnt(12)\n\t" \
    "s_waitcnt lgkmcnt(0)\n\t" \
    "v_mfma_f32_16x16x32_bf16 v[44:47], v[100:103], v[156:159], v[44:47]\n\t" \
    "ds_read_b128 v[128:131], v2 offset:64512\n\t" \
    "v_mfma_f32_16x16x32_bf16 v[72:75], v[100:103], v[160:163], v[72:75]\n\t" \
    "ds_read_b128 v[132:135], v3 offset:0\n\t" \
    "v_mfma_f32_16x16x32_bf16 v[48:51], v[104:107], v[156:159], v[48:51]\n\t" \
    "ds_read_b128 v[136:139], v3 offset:1024\n\t" \
    "v_mfma_f32_16x16x32_bf16 v[76:79], v[104:107], v[160:163], v[76:79]\n\t" \
    "ds_read_b128 v[140:143], v3 offset:2048\n\t" \
    "v_mfma_f32_16x16x32_bf16 v[52:55], v[156:159], v[108:111], v[52:55]\n\t" \
    "ds_read_b128 v[144:147], v3 offset:3072\n\t" \
    "v_mfma_f32_16x16x32_bf16 v[80:83], v[160:163], v[108:111], v[80:83]\n\t" \
    "ds_read_b128 v[148:151], v3 offset:4096\n\t" \
    "v_mfma_f32_16x16x32_bf16 v[56:59], v[156:159], v[112:115], v[56:59]\n\t" \
    "ds_read_b128 v[152:155], v3 offset:5120\n\t" \
    "v_mfma_f32_16x16x32_bf16 v[84:87], v[160:163], v[112:115], v[84:87]\n\t" \
    "v_mfma_f32_16x16x32_bf16 v[60:63], v[156:159], v[116:119], v[60:63]\n\t" \
    "global_load_dwordx4 v[212:215], v0, %[sg] offset:3072 sc1\n\t" \
    "global_load_dwordx4 v[216:219], v1, %[sg] offset:3072 sc1\n\t" \
    "v_mfma_f32_16x16x32_bf16 v[88:91], v[160:163], v[116:119], v[88:91]\n\t" \
    "v_mfma_f32_16x16x32_bf16 v[64:67], v[156:159], v[120:123], v[64:67]\n\t" \
    "v_mfma_f32_16x16x32_bf16 v[92:95], v[160:163], v[120:123], v[92:95]\n\t" \
    "v_mfma_f32_16x16x32_bf16 v[68:71], v[156:159], v[124:127], v[68:71]\n\t" \
    "v_mfma_f32_16x16x32_bf16 v[96:99], v[160:163], v[124:127], v[96:99]\n\t" \
    "s_barrier\n\t" \
    "ds_read_b32 v8, v5 offset:0\n\t" \
    "ds_read_b32 v18, v5 offset:512\n\t" \
    "ds_read_b128 v[10:13], v6 offset:0\n\t" \
    "ds_read_b32 v9, v5 offset:64\n\t" \
    "ds_read_b32 v19, v5 offset:576\n\t" \
    "ds_read_b128 v[14:17], v6 offset:64\n\t" \
    "s_waitcnt vmcnt(13)\n\t" \
    "s_waitcnt vmcnt(12)\n\t" \
    "s_waitcnt lgkmcnt(6)\n\t" \
    "v_mfma_f32_16x16x32_bf16 v[44:47], v[128:131], v[164:167], v[44:47]\n\t" \
    "ds_read_b128 v[100:103], v3 offset:6144\n\t" \
    "v_mfma_f32_16x16x32_bf16 v[72:75], v[128:131], v[168:171], v[72:75]\n\t" \
    "ds_read_b128 v[104:107], v3 offset:7168\n\t" \
    "v_mfma_f32_16x16x32_bf16 v[48:51], v[132:135], v[164:167], v[48:51]\n\t" \
    "ds_read_b128 v[108:111], v3 offset:8192\n\t" \
    "v_mfma_f32_16x16x32_bf16 v[76:79], v[132:135], v[168:171], v[76:79]\n\t" \
    "ds_read_b128 v[112:115], v3 offset:9216\n\t" \
    "v_mfma_f32_16x16x32_bf16 v[52:55], v[164:167], v[136:139], v[52:55]\n\t" \
    "ds_read_b128 v[116:119], v3 offset:10240\n\t" \
    "v_mfma_f32_16x16x32_bf16 v[80:83], v[168:171], v[136:139], v[80:83]\n\t" \
    "ds_read_b128 v[120:123], v3 offset:11264\n\t" \
    "v_mfma_f32_16x16x32_bf16 v[56:59], v[164:167], v[140:143], v[56:59]\n\t" \
    "ds_read_b128 v[124:127], v3 offset:12288\n\t" \
    "v_mfma_f32_16x16x32_bf16 v[84:87], v[168:171], v[140:143], v[84:87]\n\t" \
    "v_mfma_f32_16x16x32_bf16 v[60:63], v[164:167], v[144:147], v[60:63]\n\t" \
    "v_add_u32 v0, 8192, v0\n\t" \
    "global_load_dwordx4 v[156:159], v0, %[sg] offset:-4096 sc1\n\t" \
    "v_add_u32 v1, 8192, v1\n\t" \
    "global_load_dwordx4 v[160:163], v1, %[sg] offset:-4096 sc1\n\t" \
    "v_mfma_f32_16x16x32_bf16 v[88:91], v[168:171], v[144:147], v[88:91]\n\t" \
    "v_mfma_f32_16x16x32_bf16 v[64:67], v[164:167], v[148:151], v[64:67]\n\t" \
    "v_mfma_f32_16x16x32_bf16 v[92:95], v[168:171], v[148:151], v[92:95]\n\t" \
    "v_mfma_f32_16x16x32_bf16 v[68:71], v[164:167], v[152:155], v[68:71]\n\t" \
    "v_mfma_f32_16x16x32_bf16 v[96:99], v[168:171], v[152:155], v[96:99]\n\t" \
    "s_waitcnt vmcnt(13)\n\t" \
    "s_waitcnt vmcnt(12)\n\t" \
    "s_waitcnt lgkmcnt(0)\n\t" \
    "v_mfma_f32_16x16x32_bf16 v[44:47], v[100:103], v[172:175], v[44:47]\n\t" \
    "ds_read_b128 v[128:131], v3 offset:13312\n\t" \
    "v_mfma_f32_16x16x32_bf16 v[72:75], v[100:103], v[176:179], v[72:75]\n\t" \
    "ds_read_b128 v[132:135], v3 offset:14336\n\t" \
    "v_mfma_f32_16x16x32_bf16 v[48:51], v[104:107], v[172:175], v[48:51]\n\t" \
    "ds_read_b128 v[136:139], v3 offset:15360\n\t" \
    "v_mfma_f32_16x16x32_bf16 v[76:79], v[104:107], v[176:179], v[76:79]\n\t" \
    "ds_read_b128 v[140:143], v3 offset:16384\n\t" \
    "v_mfma_f32_16x16x32_bf16 v[52:55], v[172:175], v[108:111], v[52:55]\n\t" \
    "ds_read_b128 v[144:147], v3 offset:17408\n\t" \
    "v_mfma_f32_16x16x32_bf16 v[80:83], v[176:179], v[108:111], v[80:83]\n\t" \
    "ds_read_b128 v[148:151], v3 offset:18432\n\t" \
    "v_mfma_f32_16x16x32_bf16 v[56:59], v[172:175], v[112:115], v[56:59]\n\t" \
    "ds_read_b128 v[152:155], v3 offset:19456\n\t" \
    "v_mfma_f32_16x16x32_bf16 v[84:87], v[176:179], v[112:115], v[84:87]\n\t" \
    "v_mfma_f32_16x16x32_bf16 v[60:63], v[172:175], v[116:119], v[60:63]\n\t" \
    "global_load_dwordx4 v[164:167], v0, %[sg] offset:-3072 sc1\n\t" \
    "global_load_dwordx4 v[168:171], v1, %[sg] offset:-3072 sc1\n\t" \
    "v_mfma_f32_16x16x32_bf16 v[88:91], v[176:179], v[116:119], v[88:91]\n\t" \
    "v_mfma_f32_16x16x32_bf16 v[64:67], v[172:175], v[120:123], v[64:67]\n\t" \
    "v_mfma_f32_16x16x32_bf16 v[92:95], v[176:179], v[120:123], v[92:95]\n\t" \
    "v_mfma_f32_16x16x32_bf16 v[68:71], v[172:175], v[124:127], v[68:71]\n\t" \
    "v_mfma_f32_16x16x32_bf16 v[96:99], v[176:179], v[124:127], v[96:99]\n\t" \
    "s_mov_b64 s[44:45], 0xffffffff\n\t" \
    "v_cvt_pk_bf16_f32 v28, v18, v18\n\t" \
    "v_lshlrev_b32 v29, 16, v28\n\t" \
    "v_sub_f32 v29, v18, v29\n\t" \
    "v_cvt_pk_bf16_f32 v29, v29, v29\n\t" \
    "v_cndmask_b32 v20, v29, v28, s[44:45]\n\t" \
    "v_mov_b32 v21, v20\n\t" \
    "v_mov_b32 v22, v20\n\t" \
    "v_mov_b32 v23, v20\n\t" \
    "v_cvt_pk_bf16_f32 v28, v19, v19\n\t" \
    "v_lshlrev_b32 v29, 16, v28\n\t" \
    "v_sub_f32 v29, v19, v29\n\t" \
    "v_cvt_pk_bf16_f32 v29, v29, v29\n\t" \
    "v_cndmask_b32 v24, v29, v28, s[44:45]\n\t" \
    "v_mov_b32 v25, v24\n\t" \
    "v_mov_b32 v26, v24\n\t" \
    "v_mov_b32 v27, v24\n\t" \
    "s_waitcnt vmcnt(13)\n\t" \
    "s_waitcnt vmcnt(12)\n\t" \
    "s_waitcnt lgkmcnt(0)\n\t" \
    "v_mfma_f32_16x16x32_bf16 v[44:47], v[128:131], v[180:183], v[44:47]\n\t" \
    "ds_read_b128 v[100:103], v3 offset:20480\n\t" \
    "v_mfma_f32_16x16x32_bf16 v[72:75], v[128:131], v[184:187], v[72:75]\n\t" \
    "ds_read_b128 v[104:107], v3 offset:21504\n\t" \
    "v_mfma_f32_16x16x32_bf16 v[48:51], v[132:135], v[180:183], v[48:51]\n\t" \
    "ds_read_b128 v[108:111], v3 offset:22528\n\t" \
    "v_mfma_f32_16x16x32_bf16 v[76:79], v[132:135], v[184:187], v[76:79]\n\t" \
    "ds_read_b128 v[112:115], v3 offset:23552\n\t" \
    "v_mfma_f32_16x16x32_bf16 v[52:55], v[180:183], v[136:139], v[52:55]\n\t" \
    "ds_read_b128 v[116:119], v3 offset:24576\n\t" \
    "v_mfma_f32_16x16x32_bf16 v[80:83], v[184:187], v[136:139], v[80:83]\n\t" \
    "ds_read_b128 v[120:123], v3 offset:25600\n\t" \
    "v_mfma_f32_16x16x32_bf16 v[56:59], v[180:183], v[140:143], v[56:59]\n\t" \
    "ds_read_b128 v[124:127], v3 offset:26624\n\t" \
    "v_mfma_f32_16x16x32_bf16 v[84:87], v[184:187], v[140:143], v[84:87]\n\t" \
    "v_mfma_f32_16x16x32_bf16 v[60:63], v[180:183], v[144:147], v[60:63]\n\t" \
    "v_mfma_f32_16x16x32_bf16 v[88:91], v[184:187], v[144:147], v[88:91]\n\t" \
    "v_mfma_f32_16x16x32_bf16 v[64:67], v[180:183], v[148:151], v[64:67]\n\t" \
    "v_mfma_f32_16x16x32_bf16 v[92:95], v[184:187], v[148:151], v[92:95]\n\t" \
    "v_mfma_f32_16x16x32_bf16 v[68:71], v[180:183], v[152:155], v[68:71]\n\t" \
    "v_mfma_f32_16x16x32_bf16 v[96:99], v[184:187], v[152:155], v[96:99]\n\t" \
    "s_waitcnt vmcnt(11)\n\t" \
    "s_waitcnt vmcnt(10)\n\t" \
    "s_waitcnt lgkmcnt(0)\n\t" \
    "v_mfma_f32_16x16x32_bf16 v[44:47], v[100:103], v[188:191], v[44:47]\n\t" \
    "ds_read_b128 v[128:131], v3 offset:27648\n\t" \
    "v_mfma_f32_16x16x32_bf16 v[72:75], v[100:103], v[192:195], v[72:75]\n\t" \
    "ds_read_b128 v[132:135], v3 offset:28672\n\t" \
    "v_mfma_f32_16x16x32_bf16 v[48:51], v[104:107], v[188:191], v[48:51]\n\t" \
    "ds_read_b128 v[136:139], v3 offset:29696\n\t" \
    "v_mfma_f32_16x16x32_bf16 v[76:79], v[104:107], v[192:195], v[76:79]\n\t" \
    "ds_read_b128 v[140:143], v3 offset:30720\n\t" \
    "v_mfma_f32_16x16x32_bf16 v[52:55], v[188:191], v[108:111], v[52:55]\n\t" \
    "ds_read_b128 v[144:147], v3 offset:31744\n\t" \
    "v_mfma_f32_16x16x32_bf16 v[80:83], v[192:195], v[108:111], v[80:83]\n\t" \
    "ds_read_b128 v[148:151], v3 offset:32768\n\t" \
    "v_mfma_f32_16x16x32_bf16 v[56:59], v[188:191], v[112:115], v[56:59]\n\t" \
    "ds_read_b128 v[152:155], v3 offset:33792\n\t" \
    "v_mfma_f32_16x16x32_bf16 v[84:87], v[192:195], v[112:115], v[84:87]\n\t" \
    "v_mfma_f32_16x16x32_bf16 v[60:63], v[188:191], v[116:119], v[60:63]\n\t" \
    "v_mfma_f32_16x16x32_bf16 v[88:91], v[192:195], v[116:119], v[88:91]\n\t" \
    "v_mfma_f32_16x16x32_bf16 v[64:67], v[188:191], v[120:123], v[64:67]\n\t" \
    "v_mfma_f32_16x16x32_bf16 v[92:95], v[192:195], v[120:123], v[92:95]\n\t" \
    "v_mfma_f32_16x16x32_bf16 v[68:71], v[188:191], v[124:127], v[68:71]\n\t" \
    "v_mfma_f32_16x16x32_bf16 v[96:99], v[192:195], v[124:127], v[96:99]\n\t" \
    "s_waitcnt vmcnt(9)\n\t" \
    "s_waitcnt vmcnt(8)\n\t" \
    "s_waitcnt lgkmcnt(0)\n\t" \
    "v_mfma_f32_16x16x32_bf16 v[44:47], v[128:131], v[196:199], v[44:47]\n\t" \
    "ds_read_b128 v[100:103], v3 offset:34816\n\t" \
    "v_mfma_f32_16x16x32_bf16 v[72:75], v[128:131], v[200:203], v[72:75]\n\t" \
    "ds_read_b128 v[104:107], v3 offset:35840\n\t" \
    "v_mfma_f32_16x16x32_bf16 v[48:51], v[132:135], v[196:199], v[48:51]\n\t" \
    "ds_read_b128 v[108:111], v3 offset:36864\n\t" \
    "v_mfma_f32_16x16x32_bf16 v[76:79], v[132:135], v[200:203], v[76:79]\n\t" \
    "ds_read_b128 v[112:115], v3 offset:37888\n\t" \
    "v_mfma_f32_16x16x32_bf16 v[52:55], v[196:199], v[136:139], v[52:55]\n\t" \
    "ds_read_b128 v[116:119], v3 offset:38912\n\t" \
    "v_mfma_f32_16x16x32_bf16 v[80:83], v[200:203], v[136:139], v[80:83]\n\t" \
    "ds_read_b128 v[120:123], v3 offset:39936\n\t" \
    "v_mfma_f32_16x16x32_bf16 v[56:59], v[196:199], v[140:143], v[56:59]\n\t" \
    "ds_read_b128 v[124:127], v3 offset:40960\n\t" \
    "v_mfma_f32_16x16x32_bf16 v[84:87], v[200:203], v[140:143], v[84:87]\n\t" \
    "v_mfma_f32_16x16x32_bf16 v[60:63], v[196:199], v[144:147], v[60:63]\n\t" \
    "v_mfma_f32_16x16x32_bf16 v[88:91], v[200:203], v[144:147], v[88:91]\n\t" \
    "v_mfma_f32_16x16x32_bf16 v[64:67], v[196:199], v[148:151], v[64:67]\n\t" \
    "v_mfma_f32_16x16x32_bf16 v[92:95], v[200:203], v[148:151], v[92:95]\n\t" \
    "v_mfma_f32_16x16x32_bf16 v[68:71], v[196:199], v[152:155], v[68:71]\n\t" \
    "v_mfma_f32_16x16x32_bf16 v[96:99], v[200:203], v[152:155], v[96:99]\n\t" \
    "s_waitcnt vmcnt(7)\n\t" \
    "s_waitcnt vmcnt(6)\n\t" \
    "s_waitcnt lgkmcnt(0)\n\t" \
    "v_mfma_f32_16x16x32_bf16 v[44:47], v[100:103], v[204:207], v[44:47]\n\t" \
    "ds_read_b128 v[128:131], v3 offset:41984\n\t" \
    "v_mfma_f32_16x16x32_bf16 v[72:75], v[100:103], v[208:211], v[72:75]\n\t" \
    "ds_read_b128 v[132:135], v3 offset:43008\n\t" \
    "v_mfma_f32_16x16x32_bf16 v[48:51], v[104:107], v[204:207], v[48:51]\n\t" \
    "ds_read_b128 v[136:139], v3 offset:44032\n\t" \
    "v_mfma_f32_16x16x32_bf16 v[76:79], v[104:107], v[208:211], v[76:79]\n\t" \
    "ds_read_b128 v[140:143], v3 offset:45056\n\t" \
    "v_mfma_f32_16x16x32_bf16 v[52:55], v[204:207], v[108:111], v[52:55]\n\t" \
    "ds_read_b128 v[144:147], v3 offset:46080\n\t" \
    "v_mfma_f32_16x16x32_bf16 v[80:83], v[208:211], v[108:111], v[80:83]\n\t" \
    "ds_read_b128 v[148:151], v3 offset:47104\n\t" \
    "v_mfma_f32_16x16x32_bf16 v[56:59], v[204:207], v[112:115], v[56:59]\n\t" \
    "ds_read_b128 v[152:155], v3 offset:48128\n\t" \
    "v_mfma_f32_16x16x32_bf16 v[84:87], v[208:211], v[112:115], v[84:87]\n\t" \
    "v_mfma_f32_16x16x32_bf16 v[60:63], v[204:207], v[116:119], v[60:63]\n\t" \
    "v_mfma_f32_16x16x32_bf16 v[88:91], v[208:211], v[116:119], v[88:91]\n\t" \
    "v_mfma_f32_16x16x32_bf16 v[64:67], v[204:207], v[120:123], v[64:67]\n\t" \
    "v_mfma_f32_16x16x32_bf16 v[92:95], v[208:211], v[120:123], v[92:95]\n\t" \
    "v_mfma_f32_16x16x32_bf16 v[68:71], v[204:207], v[124:127], v[68:71]\n\t" \
    "v_mfma_f32_16x16x32_bf16 v[96:99], v[208:211], v[124:127], v[96:99]\n\t" \
    "s_waitcnt vmcnt(5)\n\t" \
    "s_waitcnt vmcnt(4)\n\t" \
    "s_waitcnt lgkmcnt(0)\n\t" \
    "v_mfma_f32_16x16x32_bf16 v[44:47], v[128:131], v[212:215], v[44:47]\n\t" \
    "ds_read_b128 v[100:103], v3 offset:49152\n\t" \
    "v_mfma_f32_16x16x32_bf16 v[72:75], v[128:131], v[216:219], v[72:75]\n\t" \
    "ds_read_b128 v[104:107], v3 offset:50176\n\t" \
    "v_mfma_f32_16x16x32_bf16 v[48:51], v[132:135], v[212:215], v[48:51]\n\t" \
    "ds_read_b128 v[108:111], v3 offset:51200\n\t" \
    "v_mfma_f32_16x16x32_bf16 v[76:79], v[132:135], v[216:219], v[76:79]\n\t" \
    "ds_read_b128 v[112:115], v3 offset:52224\n\t" \
    "v_mfma_f32_16x16x32_bf16 v[52:55], v[212:215], v[136:139], v[52:55]\n\t" \
    "ds_read_b128 v[116:119], v3 offset:53248\n\t" \
    "v_mfma_f32_16x16x32_bf16 v[80:83], v[216:219], v[136:139], v[80:83]\n\t" \
    "ds_read_b128 v[120:123], v3 offset:54272\n\t" \
    "v_mfma_f32_16x16x32_bf16 v[56:59], v[212:215], v[140:143], v[56:59]\n\t" \
    "ds_read_b128 v[124:127], v3 offset:55296\n\t" \
    "v_mfma_f32_16x16x32_bf16 v[84:87], v[216:219], v[140:143], v[84:87]\n\t" \
    "v_mfma_f32_16x16x32_bf16 v[60:63], v[212:215], v[144:147], v[60:63]\n\t" \
    "v_mfma_f32_16x16x32_bf16 v[88:91], v[216:219], v[144:147], v[88:91]\n\t" \
    "v_mfma_f32_16x16x32_bf16 v[64:67], v[212:215], v[148:151], v[64:67]\n\t" \
    "v_mfma_f32_16x16x32_bf16 v[92:95], v[216:219], v[148:151], v[92:95]\n\t" \
    "v_mfma_f32_16x16x32_bf16 v[68:71], v[212:215], v[152:155], v[68:71]\n\t" \
    "v_mfma_f32_16x16x32_bf16 v[96:99], v[216:219], v[152:155], v[96:99]\n\t" \
    "s_waitcnt vmcnt(3)\n\t" \
    "s_waitcnt vmcnt(2)\n\t" \
    "s_waitcnt lgkmcnt(0)\n\t" \
    "v_mfma_f32_16x16x32_bf16 v[44:47], v[100:103], v[156:159], v[44:47]\n\t" \
    "ds_read_b128 v[128:131], v3 offset:56320\n\t" \
    "v_mfma_f32_16x16x32_bf16 v[72:75], v[100:103], v[160:163], v[72:75]\n\t" \
    "ds_read_b128 v[132:135], v3 offset:57344\n\t" \
    "v_mfma_f32_16x16x32_bf16 v[48:51], v[104:107], v[156:159], v[48:51]\n\t" \
    "ds_read_b128 v[136:139], v3 offset:58368\n\t" \
    "v_mfma_f32_16x16x32_bf16 v[76:79], v[104:107], v[160:163], v[76:79]\n\t" \
    "ds_read_b128 v[140:143], v3 offset:59392\n\t" \
    "v_mfma_f32_16x16x32_bf16 v[52:55], v[156:159], v[108:111], v[52:55]\n\t" \
    "ds_read_b128 v[144:147], v3 offset:60416\n\t" \
    "v_mfma_f32_16x16x32_bf16 v[80:83], v[160:163], v[108:111], v[80:83]\n\t" \
    "ds_read_b128 v[148:151], v3 offset:61440\n\t" \
    "v_mfma_f32_16x16x32_bf16 v[56:59], v[156:159], v[112:115], v[56:59]\n\t" \
    "ds_read_b128 v[152:155], v3 offset:62464\n\t" \
    "v_mfma_f32_16x16x32_bf16 v[84:87], v[160:163], v[112:115], v[84:87]\n\t" \
    "v_mfma_f32_16x16x32_bf16 v[60:63], v[156:159], v[116:119], v[60:63]\n\t" \
    "v_mfma_f32_16x16x32_bf16 v[88:91], v[160:163], v[116:119], v[88:91]\n\t" \
    "v_mfma_f32_16x16x32_bf16 v[64:67], v[156:159], v[120:123], v[64:67]\n\t" \
    "v_mfma_f32_16x16x32_bf16 v[92:95], v[160:163], v[120:123], v[92:95]\n\t" \
    "v_mfma_f32_16x16x32_bf16 v[68:71], v[156:159], v[124:127], v[68:71]\n\t" \
    "v_mfma_f32_16x16x32_bf16 v[96:99], v[160:163], v[124:127], v[96:99]\n\t" \
    "s_waitcnt vmcnt(1)\n\t" \
    "s_waitcnt vmcnt(0)\n\t" \
    "s_waitcnt lgkmcnt(0)\n\t" \
    "v_mfma_f32_16x16x32_bf16 v[44:47], v[128:131], v[164:167], v[44:47]\n\t" \
    "ds_read_b128 v[100:103], v3 offset:63488\n\t" \
    "v_mfma_f32_16x16x32_bf16 v[72:75], v[128:131], v[168:171], v[72:75]\n\t" \
    "ds_read_b128 v[104:107], v3 offset:64512\n\t" \
    "v_mfma_f32_16x16x32_bf16 v[48:51], v[132:135], v[164:167], v[48:51]\n\t" \
    "ds_read_b128 v[108:111], v4 offset:0\n\t" \
    "v_mfma_f32_16x16x32_bf16 v[76:79], v[132:135], v[168:171], v[76:79]\n\t" \
    "ds_read_b128 v[112:115], v4 offset:1024\n\t" \
    "v_mfma_f32_16x16x32_bf16 v[52:55], v[164:167], v[136:139], v[52:55]\n\t" \
    "ds_read_b128 v[116:119], v4 offset:2048\n\t" \
    "v_mfma_f32_16x16x32_bf16 v[80:83], v[168:171], v[136:139], v[80:83]\n\t" \
    "ds_read_b128 v[120:123], v4 offset:3072\n\t" \
    "v_mfma_f32_16x16x32_bf16 v[56:59], v[164:167], v[140:143], v[56:59]\n\t" \
    "ds_read_b128 v[124:127], v4 offset:4096\n\t" \
    "v_mfma_f32_16x16x32_bf16 v[84:87], v[168:171], v[140:143], v[84:87]\n\t" \
    "v_mfma_f32_16x16x32_bf16 v[60:63], v[164:167], v[144:147], v[60:63]\n\t" \
    "v_mfma_f32_16x16x32_bf16 v[88:91], v[168:171], v[144:147], v[88:91]\n\t" \
    "v_mfma_f32_16x16x32_bf16 v[64:67], v[164:167], v[148:151], v[64:67]\n\t" \
    "v_mfma_f32_16x16x32_bf16 v[92:95], v[168:171], v[148:151], v[92:95]\n\t" \
    "v_mfma_f32_16x16x32_bf16 v[68:71], v[164:167], v[152:155], v[68:71]\n\t" \
    "v_mfma_f32_16x16x32_bf16 v[96:99], v[168:171], v[152:155], v[96:99]\n\t" \
    "s_waitcnt lgkmcnt(0)\n\t" \
    "v_mfma_f32_16x16x32_bf16 v[44:47], v[100:103], v[20:23], v[44:47]\n\t" \
    "v_mfma_f32_16x16x32_bf16 v[72:75], v[100:103], v[24:27], v[72:75]\n\t" \
    "v_mfma_f32_16x16x32_bf16 v[48:51], v[104:107], v[20:23], v[48:51]\n\t" \
    "v_mfma_f32_16x16x32_bf16 v[76:79], v[104:107], v[24:27], v[76:79]\n\t" \
    "v_mfma_f32_16x16x32_bf16 v[52:55], v[20:23], v[108:111], v[52:55]\n\t" \
    "v_mfma_f32_16x16x32_bf16 v[80:83], v[24:27], v[108:111], v[80:83]\n\t" \
    "v_mfma_f32_16x16x32_bf16 v[56:59], v[20:23], v[112:115], v[56:59]\n\t" \
    "v_mfma_f32_16x16x32_bf16 v[84:87], v[24:27], v[112:115], v[84:87]\n\t" \
    "v_mfma_f32_16x16x32_bf16 v[60:63], v[20:23], v[116:119], v[60:63]\n\t" \
    "v_mfma_f32_16x16x32_bf16 v[88:91], v[24:27], v[116:119], v[88:91]\n\t" \
    "v_mfma_f32_16x16x32_bf16 v[64:67], v[20:23], v[120:123], v[64:67]\n\t" \
    "v_mfma_f32_16x16x32_bf16 v[92:95], v[24:27], v[120:123], v[92:95]\n\t" \
    "v_mfma_f32_16x16x32_bf16 v[68:71], v[20:23], v[124:127], v[68:71]\n\t" \
    "v_mfma_f32_16x16x32_bf16 v[96:99], v[24:27], v[124:127], v[96:99]\n\t" \
    "ds_write_b128 v7, v[44:47] offset:0\n\t" \
    "ds_write_b128 v7, v[48:51] offset:1024\n\t" \
    "ds_write_b128 v7, v[72:75] offset:2048\n\t" \
    "ds_write_b128 v7, v[76:79] offset:3072\n\t" \
    "s_waitcnt lgkmcnt(0)\n\t" \
    "s_barrier\n\t" \
    "s_nop 1\n\t"
#define QKV_ASM_CB \
    "global_load_dwordx4 v[156:159], v0, %[sg] offset:-4096 sc1\n\t" \
    "global_load_dwordx4 v[160:163], v1, %[sg] offset:-4096 sc1\n\t" \
    "global_load_dwordx4 v[164:167], v0, %[sg] offset:-3072 sc1\n\t" \
    "global_load_dwordx4 v[168:171], v1, %[sg] offset:-3072 sc1\n\t" \
    "global_load_dwordx4 v[172:175], v0, %[sg] offset:-2048 sc1\n\t" \
    "global_load_dwordx4 v[176:179], v1, %[sg] offset:-2048 sc1\n\t" \
    "global_load_dwordx4 v[180:183], v0, %[sg] offset:-1024 sc1\n\t" \
    "global_load_dwordx4 v[184:187], v1, %[sg] offset:-1024 sc1\n\t" \
    "v_mov_b32 v237, 1\n\t" \
    "s_waitcnt vmcnt(8)\n\t" \
    "ds_add_u32 v236, v237\n\t" \
    "ds_read_b32 v237, v236 offset:0\n\t" \
    "s_waitcnt lgkmcnt(0)\n\t" \
    "22:\n\t" \
    "v_readfirstlane_b32 s46, v237\n\t" \
    "s_cmp_ge_u32 s46, %[wb]\n\t" \
    "s_cbranch_scc1 23f\n\t" \
    "s_sleep 1\n\t" \
    "ds_read_b32 v237, v236 offset:0\n\t" \
    "s_waitcnt lgkmcnt(0)\n\t" \
    "s_branch 22b\n\t" \
    "23:\n\t" \
    "ds_read_b128 v[100:103], v2 offset:0\n\t" \
    "ds_read_b128 v[104:107], v2 offset:1024\n\t" \
    "ds_read_b128 v[108:111], v2 offset:2048\n\t" \
    "ds_read_b128 v[112:115], v2 offset:3072\n\t" \
    "ds_read_b128 v[116:119], v2 offset:4096\n\t" \
    "ds_read_b128 v[120:123], v2 offset:5120\n\t" \
    "ds_read_b128 v[124:127], v2 offset:6144\n\t" \
    "s_waitcnt vmcnt(7)\n\t" \
    "s_waitcnt vmcnt(6)\n\t" \
    "s_waitcnt lgkmcnt(0)\n\t" \
    "v_mfma_f32_16x16x32_bf16 v[44:47], v[100:103], v[156:159], 0\n\t" \
    "ds_read_b128 v[128:131], v2 offset:7168\n\t" \
    "v_mfma_f32_16x16x32_bf16 v[72:75], v[100:103], v[160:163], 0\n\t" \
    "ds_read_b128 v[132:135], v2 offset:8192\n\t" \
    "v_mfma_f32_16x16x32_bf16 v[48:51], v[104:107], v[156:159], 0\n\t" \
    "ds_read_b128 v[136:139], v2 offset:9216\n\t" \
    "v_mfma_f32_16x16x32_bf16 v[76:79], v[104:107], v[160:163], 0\n\t" \
    "ds_read_b128 v[140:143], v2 offset:10240\n\t" \
    "v_mfma_f32_16x16x32_bf16 v[52:55], v[108:111], v[156:159], 0\n\t" \
    "ds_read_b128 v[144:147], v2 offset:11264\n\t" \
    "v_mfma_f32_16x16x32_bf16 v[80:83], v[108:111], v[160:163], 0\n\t" \
    "ds_read_b128 v[148:151], v2 offset:12288\n\t" \
    "v_mfma_f32_16x16x32_bf16 v[56:59], v[112:115], v[156:159], 0\n\t" \
    "ds_read_b128 v[152:155], v2 offset:13312\n\t" \
    "v_mfma_f32_16x16x32_bf16 v[84:87], v[112:115], v[160:163], 0\n\t" \
    "v_mfma_f32_16x16x32_bf16 v[60:63], v[156:159], v[116:119], 0\n\t" \
    "global_load_dwordx4 v[188:191], v0, %[sg] offset:0 sc1\n\t" \
    "global_load_dwordx4 v[192:195], v1, %[sg] offset:0 sc1\n\t" \
    "v_mfma_f32_16x16x32_bf16 v[88:91], v[160:163], v[116:119], 0\n\t" \
    "v_mfma_f32_16x16x32_bf16 v[64:67], v[156:159], v[120:123], 0\n\t" \
    "global_load_dwordx4 v[196:199], v0, %[sg] offset:1024 sc1\n\t" \
    "global_load_dwordx4 v[200:203], v1, %[sg] offset:1024 sc1\n\t" \
    "v_mfma_f32_16x16x32_bf16 v[92:95], v[160:163], v[120:123], 0\n\t" \
    "v_mfma_f32_16x16x32_bf16 v[68:71], v[156:159], v[124:127], 0\n\t" \
    "v_mfma_f32_16x16x32_bf16 v[96:99], v[160:163], v[124:127], 0\n\t" \
    "s_waitcnt vmcnt(9)\n\t" \
    "s_waitcnt vmcnt(8)\n\t" \
    "s_waitcnt lgkmcnt(0)\n\t" \
    "v_mfma_f32_16x16x32_bf16 v[44:47], v[128:131], v[164:167], v[44:47]\n\t" \
    "ds_read_b128 v[100:103], v2 offset:14336\n\t" \
    "v_mfma_f32_16x16x32_bf16 v[72:75], v[128:131], v[168:171], v[72:75]\n\t" \
    "ds_read_b128 v[104:107], v2 offset:15360\n\t" \
    "v_mfma_f32_16x16x32_bf16 v[48:51], v[132:135], v[164:167], v[48:51]\n\t" \
    "ds_read_b128 v[108:111], v2 offset:16384\n\t" \
    "v_mfma_f32_16x16x32_bf16 v[76:79], v[132:135], v[168:171], v[76:79]\n\t" \
    "ds_read_b128 v[112:115], v2 offset:17408\n\t" \
    "v_mfma_f32_16x16x32_bf16 v[52:55], v[136:139], v[164:167], v[52:55]\n\t" \
    "ds_read_b128 v[116:119], v2 offset:18432\n\t" \
    "v_mfma_f32_16x16x32_bf16 v[80:83], v[136:139], v[168:171], v[80:83]\n\t" \
    "ds_read_b128 v[120:123], v2 offset:19456\n\t" \
    "v_mfma_f32_16x16x32_bf16 v[56:59], v[140:143], v[164:167], v[56:59]\n\t" \
    "ds_read_b128 v[124:127], v2 offset:20480\n\t" \
    "v_mfma_f32_16x16x32_bf16 v[84:87], v[140:143], v[168:171], v[84:87]\n\t" \
    "v_mfma_f32_16x16x32_bf16 v[60:63], v[164:167], v[144:147], v[60:63]\n\t" \
    "global_load_dwordx4 v[204:207], v0, %[sg] offset:2048 sc1\n\t" \
    "global_load_dwordx4 v[208:211], v1, %[sg] offset:2048 sc1\n\t" \
    "v_mfma_f32_16x16x32_bf16 v[88:91], v[168:171], v[144:147], v[88:91]\n\t" \
    "v_mfma_f32_16x16x32_bf16 v[64:67], v[164:167], v[148:151], v[64:67]\n\t" \
    "global_load_dwordx4 v[212:215], v0, %[sg] offset:3072 sc1\n\t" \
    "global_load_dwordx4 v[216:219], v1, %[sg] offset:3072 sc1\n\t" \
    "v_mfma_f32_16x16x32_bf16 v[92:95], v[168:171], v[148:151], v[92:95]\n\t" \
    "v_mfma_f32_16x16x32_bf16 v[68:71], v[164:167], v[152:155], v[68:71]\n\t" \
    "v_mfma_f32_16x16x32_bf16 v[96:99], v[168:171], v[152:155], v[96:99]\n\t" \
    "s_waitcnt vmcnt(11)\n\t" \
    "s_waitcnt vmcnt(10)\n\t" \
    "s_waitcnt lgkmcnt(0)\n\t" \
    "v_mfma_f32_16x16x32_bf16 v[44:47], v[100:103], v[172:175], v[44:47]\n\t" \
    "ds_read_b128 v[128:131], v2 offset:21504\n\t" \
    "v_mfma_f32_16x16x32_bf16 v[72:75], v[100:103], v[176:179], v[72:75]\n\t" \
    "ds_read_b128 v[132:135], v2 offset:22528\n\t" \
    "v_mfma_f32_16x16x32_bf16 v[48:51], v[104:107], v[172:175], v[48:51]\n\t" \
    "ds_read_b128 v[136:139], v2 offset:23552\n\t" \
    "v_mfma_f32_16x16x32_bf16 v[76:79], v[104:107], v[176:179], v[76:79]\n\t" \
    "ds_read_b128 v[140:143], v2 offset:24576\n\t" \
    "v_mfma_f32_16x16x32_bf16 v[52:55], v[108:111], v[172:175], v[52:55]\n\t" \
    "ds_read_b128 v[144:147], v2 offset:25600\n\t" \
    "v_mfma_f32_16x16x32_bf16 v[80:83], v[108:111], v[176:179], v[80:83]\n\t" \
    "ds_read_b128 v[148:151], v2 offset:26624\n\t" \
    "v_mfma_f32_16x16x32_bf16 v[56:59], v[112:115], v[172:175], v[56:59]\n\t" \
    "ds_read_b128 v[152:155], v2 offset:27648\n\t" \
    "v_mfma_f32_16x16x32_bf16 v[84:87], v[112:115], v[176:179], v[84:87]\n\t" \
    "v_mfma_f32_16x16x32_bf16 v[60:63], v[172:175], v[116:119], v[60:63]\n\t" \
    "v_add_u32 v0, 8192, v0\n\t" \
    "global_load_dwordx4 v[156:159], v0, %[sg] offset:-4096 sc1\n\t" \
    "v_add_u32 v1, 8192, v1\n\t" \
    "global_load_dwordx4 v[160:163], v1, %[sg] offset:-4096 sc1\n\t" \
    "v_mfma_f32_16x16x32_bf16 v[88:91], v[176:179], v[116:119], v[88:91]\n\t" \
    "v_mfma_f32_16x16x32_bf16 v[64:67], v[172:175], v[120:123], v[64:67]\n\t" \
    "global_load_dwordx4 v[164:167], v0, %[sg] offset:-3072 sc1\n\t" \
    "global_load_dwordx4 v[168:171], v1, %[sg] offset:-3072 sc1\n\t" \
    "v_mfma_f32_16x16x32_bf16 v[92:95], v[176:179], v[120:123], v[92:95]\n\t" \
    "v_mfma_f32_16x16x32_bf16 v[68:71], v[172:175], v[124:127], v[68:71]\n\t" \
    "v_mfma_f32_16x16x32_bf16 v[96:99], v[176:179], v[124:127], v[96:99]\n\t" \
    "s_waitcnt vmcnt(13)\n\t" \
    "s_waitcnt vmcnt(12)\n\t" \
    "s_waitcnt lgkmcnt(0)\n\t" \
    "v_mfma_f32_16x16x32_bf16 v[44:47], v[128:131], v[180:183], v[44:47]\n\t" \
    "ds_read_b128 v[100:103], v2 offset:28672\n\t" \
    "v_mfma_f32_16x16x32_bf16 v[72:75], v[128:131], v[184:187], v[72:75]\n\t" \
    "ds_read_b128 v[104:107], v2 offset:29696\n\t" \
    "v_mfma_f32_16x16x32_bf16 v[48:51], v[132:135], v[180:183], v[48:51]\n\t" \
    "ds_read_b128 v[108:111], v2 offset:30720\n\t" \
    "v_mfma_f32_16x16x32_bf16 v[76:79], v[132:135], v[184:187], v[76:79]\n\t" \
    "ds_read_b128 v[112:115], v2 offset:31744\n\t" \
    "v_mfma_f32_16x16x32_bf16 v[52:55], v[136:139], v[180:183], v[52:55]\n\t" \
    "ds_read_b128 v[116:119], v2 offset:32768\n\t" \
    "v_mfma_f32_16x16x32_bf16 v[80:83], v[136:139], v[184:187], v[80:83]\n\t" \
    "ds_read_b128 v[120:123], v2 offset:33792\n\t" \
    "v_mfma_f32_16x16x32_bf16 v[56:59], v[140:143], v[180:183], v[56:59]\n\t" \
    "ds_read_b128 v[124:127], v2 offset:34816\n\t" \
    "v_mfma_f32_16x16x32_bf16 v[84:87], v[140:143], v[184:187], v[84:87]\n\t" \
    "v_mfma_f32_16x16x32_bf16 v[60:63], v[180:183], v[144:147], v[60:63]\n\t" \
    "global_load_dwordx4 v[172:175], v0, %[sg] offset:-2048 sc1\n\t" \
    "global_load_dwordx4 v[176:179], v1, %[sg] offset:-2048 sc1\n\t" \
    "v_mfma_f32_16x16x32_bf16 v[88:91], v[184:187], v[144:147], v[88:91]\n\t" \
    "v_mfma_f32_16x16x32_bf16 v[64:67], v[180:183], v[148:151], v[64:67]\n\t" \
    "v_mfma_f32_16x16x32_bf16 v[92:95], v[184:187], v[148:151], v[92:95]\n\t" \
    "v_mfma_f32_16x16x32_bf16 v[68:71], v[180:183], v[152:155], v[68:71]\n\t" \
    "v_mfma_f32_16x16x32_bf16 v[96:99], v[184:187], v[152:155], v[96:99]\n\t" \
    "s_waitcnt vmcnt(13)\n\t" \
    "s_waitcnt vmcnt(12)\n\t" \
    "s_waitcnt lgkmcnt(0)\n\t" \
    "v_mfma_f32_16x16x32_bf16 v[44:47], v[100:103], v[188:191], v[44:47]\n\t" \
    "ds_read_b128 v[128:131], v2 offset:35840\n\t" \
    "v_mfma_f32_16x16x32_bf16 v[72:75], v[100:103], v[192:195], v[72:75]\n\t" \
    "ds_read_b128 v[132:135], v2 offset:36864\n\t" \
    "v_mfma_f32_16x16x32_bf16 v[48:51], v[104:107], v[188:191], v[48:51]\n\t" \
    "ds_read_b128 v[136:139], v2 offset:37888\n\t" \
    "v_mfma_f32_16x16x32_bf16 v[76:79], v[104:107], v[192:195], v[76:79]\n\t" \
    "ds_read_b128 v[140:143], v2 offset:38912\n\t" \
    "v_mfma_f32_16x16x32_bf16 v[52:55], v[108:111], v[188:191], v[52:55]\n\t" \
    "ds_read_b128 v[144:147], v2 offset:39936\n\t" \
    "v_mfma_f32_16x16x32_bf16 v[80:83], v[108:111], v[192:195], v[80:83]\n\t" \
    "ds_read_b128 v[148:151], v2 offset:40960\n\t" \
    "v_mfma_f32_16x16x32_bf16 v[56:59], v[112:115], v[188:191], v[56:59]\n\t" \
    "ds_read_b128 v[152:155], v2 offset:41984\n\t" \
    "v_mfma_f32_16x16x32_bf16 v[84:87], v[112:115], v[192:195], v[84:87]\n\t" \
    "v_mfma_f32_16x16x32_bf16 v[60:63], v[188:191], v[116:119], v[60:63]\n\t" \
    "global_load_dwordx4 v[180:183], v0, %[sg] offset:-1024 sc1\n\t" \
    "global_load_dwordx4 v[184:187], v1, %[sg] offset:-1024 sc1\n\t" \
    "v_mfma_f32_16x16x32_bf16 v[88:91], v[192:195], v[116:119], v[88:91]\n\t" \
    "v_mfma_f32_16x16x32_bf16 v[64:67], v[188:191], v[120:123], v[64:67]\n\t" \
    "v_mfma_f32_16x16x32_bf16 v[92:95], v[192:195], v[120:123], v[92:95]\n\t" \
    "v_mfma_f32_16x16x32_bf16 v[68:71], v[188:191], v[124:127], v[68:71]\n\t" \
    "v_mfma_f32_16x16x32_bf16 v[96:99], v[192:195], v[124:127], v[96:99]\n\t" \
    "s_waitcnt vmcnt(13)\n\t" \
    "s_waitcnt vmcnt(12)\n\t" \
    "s_waitcnt lgkmcnt(0)\n\t" \
    "v_mfma_f32_16x16x32_bf16 v[44:47], v[128:131], v[196:199], v[44:47]\n\t" \
    "ds_read_b128 v[100:103], v2 offset:43008\n\t" \
    "v_mfma_f32_16x16x32_bf16 v[72:75], v[128:131], v[200:203], v[72:75]\n\t" \
    "ds_read_b128 v[104:107], v2 offset:44032\n\t" \
    "v_mfma_f32_16x16x32_bf16 v[48:51], v[132:135], v[196:199], v[48:51]\n\t" \
    "ds_read_b128 v[108:111], v2 offset:45056\n\t" \
    "v_mfma_f32_16x16x32_bf16 v[76:79], v[132:135], v[200:203], v[76:79]\n\t" \
    "ds_read_b128 v[112:115], v2 offset:46080\n\t" \
    "v_mfma_f32_16x16x32_bf16 v[52:55], v[136:139], v[196:199], v[52:55]\n\t" \
    "ds_read_b128 v[116:119], v2 offset:47104\n\t" \
    "v_mfma_f32_16x16x32_bf16 v[80:83], v[136:139], v[200:203], v[80:83]\n\t" \
    "ds_read_b128 v[120:123], v2 offset:48128\n\t" \
    "v_mfma_f32_16x16x32_bf16 v[56:59], v[140:143], v[196:199], v[56:59]\n\t" \
    "ds_read_b128 v[124:127], v2 offset:49152\n\t" \
    "v_mfma_f32_16x16x32_bf16 v[84:87], v[140:143], v[200:203], v[84:87]\n\t" \
    "v_mfma_f32_16x16x32_bf16 v[60:63], v[196:199], v[144:147], v[60:63]\n\t" \
    "global_load_dwordx4 v[188:191], v0, %[sg] offset:0 sc1\n\t" \
    "global_load_dwordx4 v[192:195], v1, %[sg] offset:0 sc1\n\t" \
    "v_mfma_f32_16x16x32_bf16 v[88:91], v[200:203], v[144:147], v[88:91]\n\t" \
    "v_mfma_f32_16x16x32_bf16 v[64:67], v[196:199], v[148:151], v[64:67]\n\t" \
    "v_mfma_f32_16x16x32_bf16 v[92:95], v[200:203], v[148:151], v[92:95]\n\t" \
    "v_mfma_f32_16x16x32_bf16 v[68:71], v[196:199], v[152:155], v[68:71]\n\t" \
    "v_mfma_f32_16x16x32_bf16 v[96:99], v[200:203], v[152:155], v[96:99]\n\t" \
    "s_waitcnt vmcnt(13)\n\t" \
    "s_waitcnt vmcnt(12)\n\t" \
    "s_waitcnt lgkmcnt(0)\n\t" \
    "v_mfma_f32_16x16x32_bf16 v[44:47], v[100:103], v[204:207], v[44:47]\n\t" \
    "ds_read_b128 v[128:131], v2 offset:50176\n\t" \
    "v_mfma_f32_16x16x32_bf16 v[72:75], v[100:103], v[208:211], v[72:75]\n\t" \
    "ds_read_b128 v[132:135], v2 offset:51200\n\t" \
    "v_mfma_f32_16x16x32_bf16 v[48:51], v[104:107], v[204:207], v[48:51]\n\t" \
    "ds_read_b128 v[136:139], v2 offset:52224\n\t" \
    "v_mfma_f32_16x16x32_bf16 v[76:79], v[104:107], v[208:211], v[76:79]\n\t" \
    "ds_read_b128 v[140:143], v2 offset:53248\n\t" \
    "v_mfma_f32_16x16x32_bf16 v[52:55], v[108:111], v[204:207], v[52:55]\n\t" \
    "ds_read_b128 v[144:147], v2 offset:54272\n\t" \
    "v_mfma_f32_16x16x32_bf16 v[80:83], v[108:111], v[208:211], v[80:83]\n\t" \
    "ds_read_b128 v[148:151], v2 offset:55296\n\t" \
    "v_mfma_f32_16x16x32_bf16 v[56:59], v[112:115], v[204:207], v[56:59]\n\t" \
    "ds_read_b128 v[152:155], v2 offset:56320\n\t" \
    "v_mfma_f32_16x16x32_bf16 v[84:87], v[112:115], v[208:211], v[84:87]\n\t" \
    "v_mfma_f32_16x16x32_bf16 v[60:63], v[204:207], v[116:119], v[60:63]\n\t" \
    "global_load_dwordx4 v[196:199], v0, %[sg] offset:1024 sc1\n\t" \
    "global_load_dwordx4 v[200:203], v1, %[sg] offset:1024 sc1\n\t" \
    "v_mfma_f32_16x16x32_bf16 v[88:91], v[208:211], v[116:119], v[88:91]\n\t" \
    "v_mfma_f32_16x16x32_bf16 v[64:67], v[204:207], v[120:123], v[64:67]\n\t" \
    "v_mfma_f32_16x16x32_bf16 v[92:95], v[208:211], v[120:123], v[92:95]\n\t" \
    "v_mfma_f32_16x16x32_bf16 v[68:71], v[204:207], v[124:127], v[68:71]\n\t" \
    "v_mfma_f32_16x16x32_bf16 v[96:99], v[208:211], v[124:127], v[96:99]\n\t" \
    "s_waitcnt vmcnt(13)\n\t" \
    "s_waitcnt vmcnt(12)\n\t" \
    "s_waitcnt lgkmcnt(0)\n\t" \
    "v_mfma_f32_16x16x32_bf16 v[44:47], v[128:131], v[212:215], v[44:47]\n\t" \
    "ds_read_b128 v[100:103], v2 offset:57344\n\t" \
    "v_mfma_f32_16x16x32_bf16 v[72:75], v[128:131], v[216:219], v[72:75]\n\t" \
    "ds_read_b128 v[104:107], v2 offset:58368\n\t" \
    "v_mfma_f32_16x16x32_bf16 v[48:51], v[132:135], v[212:215], v[48:51]\n\t" \
    "ds_read_b128 v[108:111], v2 offset:59392\n\t" \
    "v_mfma_f32_16x16x32_bf16 v[76:79], v[132:135], v[216:219], v[76:79]\n\t" \
    "ds_read_b128 v[112:115], v2 offset:60416\n\t" \
    "v_mfma_f32_16x16x32_bf16 v[52:55], v[136:139], v[212:215], v[52:55]\n\t" \
    "ds_read_b128 v[116:119], v2 offset:61440\n\t" \
    "v_mfma_f32_16x16x32_bf16 v[80:83], v[136:139], v[216:219], v[80:83]\n\t" \
    "ds_read_b128 v[120:123], v2 offset:62464\n\t" \
    "v_mfma_f32_16x16x32_bf16 v[56:59], v[140:143], v[212:215], v[56:59]\n\t" \
    "ds_read_b128 v[124:127], v2 offset:63488\n\t" \
    "v_mfma_f32_16x16x32_bf16 v[84:87], v[140:143], v[216:219], v[84:87]\n\t" \
    "v_mfma_f32_16x16x32_bf16 v[60:63], v[212:215], v[144:147], v[60:63]\n\t" \
    "global_load_dwordx4 v[204:207], v0, %[sg] offset:2048 sc1\n\t" \
    "global_load_dwordx4 v[208:211], v1, %[sg] offset:2048 sc1\n\t" \
    "v_mfma_f32_16x16x32_bf16 v[88:91], v[216:219], v[144:147], v[88:91]\n\t" \
    "v_mfma_f32_16x16x32_bf16 v[64:67], v[212:215], v[148:151], v[64:67]\n\t" \
    "v_mfma_f32_16x16x32_bf16 v[92:95], v[216:219], v[148:151], v[92:95]\n\t" \
    "v_mfma_f32_16x16x32_bf16 v[68:71], v[212:215], v[152:155], v[68:71]\n\t" \
    "v_mfma_f32_16x16x32_bf16 v[96:99], v[216:219], v[152:155], v[96:99]\n\t" \
    "s_waitcnt vmcnt(13)\n\t" \
    "s_waitcnt vmcnt(12)\n\t" \
    "s_waitcnt lgkmcnt(0)\n\t" \
    "v_mfma_f32_16x16x32_bf16 v[44:47], v[100:103], v[156:159], v[44:47]\n\t" \
    "ds_read_b128 v[128:131], v2 offset:64512\n\t" \
    "v_mfma_f32_16x16x32_bf16 v[72:75], v[100:103], v[160:163], v[72:75]\n\t" \
    "ds_read_b128 v[132:135], v3 offset:0\n\t" \
    "v_mfma_f32_16x16x32_bf16 v[48:51], v[104:107], v[156:159], v[48:51]\n\t" \
    "ds_read_b128 v[136:139], v3 offset:1024\n\t" \
    "v_mfma_f32_16x16x32_bf16 v[76:79], v[104:107], v[160:163], v[76:79]\n\t" \
    "ds_read_b128 v[140:143], v3 offset:2048\n\t" \
    "v_mfma_f32_16x16x32_bf16 v[52:55], v[108:111], v[156:159], v[52:55]\n\t" \
    "ds_read_b128 v[144:147], v3 offset:3072\n\t" \
    "v_mfma_f32_16x16x32_bf16 v[80:83], v[108:111], v[160:163], v[80:83]\n\t" \
    "ds_read_b128 v[148:151], v3 offset:4096\n\t" \
    "v_mfma_f32_16x16x32_bf16 v[56:59], v[112:115], v[156:159], v[56:59]\n\t" \
    "ds_read_b128 v[152:155], v3 offset:5120\n\t" \
    "v_mfma_f32_16x16x32_bf16 v[84:87], v[112:115], v[160:163], v[84:87]\n\t" \
    "v_mfma_f32_16x16x32_bf16 v[60:63], v[156:159], v[116:119], v[60:63]\n\t" \
    "global_load_dwordx4 v[212:215], v0, %[sg] offset:3072 sc1\n\t" \
    "global_load_dwordx4 v[216:219], v1, %[sg] offset:3072 sc1\n\t" \
    "v_mfma_f32_16x16x32_bf16 v[88:91], v[160:163], v[116:119], v[88:91]\n\t" \
    "v_mfma_f32_16x16x32_bf16 v[64:67], v[156:159], v[120:123], v[64:67]\n\t" \
    "v_mfma_f32_16x16x32_bf16 v[92:95], v[160:163], v[120:123], v[92:95]\n\t" \
    "v_mfma_f32_16x16x32_bf16 v[68:71], v[156:159], v[124:127], v[68:71]\n\t" \
    "v_mfma_f32_16x16x32_bf16 v[96:99], v[160:163], v[124:127], v[96:99]\n\t" \
    "s_barrier\n\t" \
    "ds_read_b32 v8, v5 offset:0\n\t" \
    "ds_read_b32 v18, v5 offset:512\n\t" \
    "ds_read_b128 v[10:13], v6 offset:0\n\t" \
    "ds_read_b32 v9, v5 offset:64\n\t" \
    "ds_read_b32 v19, v5 offset:576\n\t" \
    "ds_read_b128 v[14:17], v6 offset:64\n\t" \
    "s_waitcnt vmcnt(13)\n\t" \
    "s_waitcnt vmcnt(12)\n\t" \
    "s_waitcnt lgkmcnt(6)\n\t" \
    "v_mfma_f32_16x16x32_bf16 v[44:47], v[128:131], v[164:167], v[44:47]\n\t" \
    "ds_read_b128 v[100:103], v3 offset:6144\n\t" \
    "v_mfma_f32_16x16x32_bf16 v[72:75], v[128:131], v[168:171], v[72:75]\n\t" \
    "ds_read_b128 v[104:107], v3 offset:7168\n\t" \
    "v_mfma_f32_16x16x32_bf16 v[48:51], v[132:135], v[164:167], v[48:51]\n\t" \
    "ds_read_b128 v[108:111], v3 offset:8192\n\t" \
    "v_mfma_f32_16x16x32_bf16 v[76:79], v[132:135], v[168:171], v[76:79]\n\t" \
    "ds_read_b128 v[112:115], v3 offset:9216\n\t" \
    "v_mfma_f32_16x16x32_bf16 v[52:55], v[136:139], v[164:167], v[52:55]\n\t" \
    "ds_read_b128 v[116:119], v3 offset:10240\n\t" \
    "v_mfma_f32_16x16x32_bf16 v[80:83], v[136:139], v[168:171], v[80:83]\n\t" \
    "ds_read_b128 v[120:123], v3 offset:11264\n\t" \
    "v_mfma_f32_16x16x32_bf16 v[56:59], v[140:143], v[164:167], v[56:59]\n\t" \
    "ds_read_b128 v[124:127], v3 offset:12288\n\t" \
    "v_mfma_f32_16x16x32_bf16 v[84:87], v[140:143], v[168:171], v[84:87]\n\t" \
    "v_mfma_f32_16x16x32_bf16 v[60:63], v[164:167], v[144:147], v[60:63]\n\t" \
    "v_add_u32 v0, 8192, v0\n\t" \
    "global_load_dwordx4 v[156:159], v0, %[sg] offset:-4096 sc1\n\t" \
    "v_add_u32 v1, 8192, v1\n\t" \
    "global_load_dwordx4 v[160:163], v1, %[sg] offset:-4096 sc1\n\t" \
    "v_mfma_f32_16x16x32_bf16 v[88:91], v[168:171], v[144:147], v[88:91]\n\t" \
    "v_mfma_f32_16x16x32_bf16 v[64:67], v[164:167], v[148:151], v[64:67]\n\t" \
    "v_mfma_f32_16x16x32_bf16 v[92:95], v[168:171], v[148:151], v[92:95]\n\t" \
    "v_mfma_f32_16x16x32_bf16 v[68:71], v[164:167], v[152:155], v[68:71]\n\t" \
    "v_mfma_f32_16x16x32_bf16 v[96:99], v[168:171], v[152:155], v[96:99]\n\t" \
    "s_waitcnt vmcnt(13)\n\t" \
    "s_waitcnt vmcnt(12)\n\t" \
    "s_waitcnt lgkmcnt(0)\n\t" \
    "v_mfma_f32_16x16x32_bf16 v[44:47], v[100:103], v[172:175], v[44:47]\n\t" \
    "ds_read_b128 v[128:131], v3 offset:13312\n\t" \
    "v_mfma_f32_16x16x32_bf16 v[72:75], v[100:103], v[176:179], v[72:75]\n\t" \
    "ds_read_b128 v[132:135], v3 offset:14336\n\t" \
    "v_mfma_f32_16x16x32_bf16 v[48:51], v[104:107], v[172:175], v[48:51]\n\t" \
    "ds_read_b128 v[136:139], v3 offset:15360\n\t" \
    "v_mfma_f32_16x16x32_bf16 v[76:79], v[104:107], v[176:179], v[76:79]\n\t" \
    "ds_read_b128 v[140:143], v3 offset:16384\n\t" \
    "v_mfma_f32_16x16x32_bf16 v[52:55], v[108:111], v[172:175], v[52:55]\n\t" \
    "ds_read_b128 v[144:147], v3 offset:17408\n\t" \
    "v_mfma_f32_16x16x32_bf16 v[80:83], v[108:111], v[176:179], v[80:83]\n\t" \
    "ds_read_b128 v[148:151], v3 offset:18432\n\t" \
    "v_mfma_f32_16x16x32_bf16 v[56:59], v[112:115], v[172:175], v[56:59]\n\t" \
    "ds_read_b128 v[152:155], v3 offset:19456\n\t" \
    "v_mfma_f32_16x16x32_bf16 v[84:87], v[112:115], v[176:179], v[84:87]\n\t" \
    "v_mfma_f32_16x16x32_bf16 v[60:63], v[172:175], v[116:119], v[60:63]\n\t" \
    "global_load_dwordx4 v[164:167], v0, %[sg] offset:-3072 sc1\n\t" \
    "global_load_dwordx4 v[168:171], v1, %[sg] offset:-3072 sc1\n\t" \
    "v_mfma_f32_16x16x32_bf16 v[88:91], v[176:179], v[116:119], v[88:91]\n\t" \
    "v_mfma_f32_16x16x32_bf16 v[64:67], v[172:175], v[120:123], v[64:67]\n\t" \
    "v_mfma_f32_16x16x32_bf16 v[92:95], v[176:179], v[120:123], v[92:95]\n\t" \
    "v_mfma_f32_16x16x32_bf16 v[68:71], v[172:175], v[124:127], v[68:71]\n\t" \
    "v_mfma_f32_16x16x32_bf16 v[96:99], v[176:179], v[124:127], v[96:99]\n\t" \
    "s_mov_b64 s[44:45], 0xffffffff\n\t" \
    "v_cvt_pk_bf16_f32 v28, v18, v18\n\t" \
    "v_lshlrev_b32 v29, 16, v28\n\t" \
    "v_sub_f32 v29, v18, v29\n\t" \
    "v_cvt_pk_bf16_f32 v29, v29, v29\n\t" \
    "v_cndmask_b32 v20, v29, v28, s[44:45]\n\t" \
    "v_mov_b32 v21, v20\n\t" \
    "v_mov_b32 v22, v20\n\t" \
    "v_mov_b32 v23, v20\n\t" \
    "v_cvt_pk_bf16_f32 v28, v19, v19\n\t" \
    "v_lshlrev_b32 v29, 16, v28\n\t" \
    "v_sub_f32 v29, v19, v29\n\t" \
    "v_cvt_pk_bf16_f32 v29, v29, v29\n\t" \
    "v_cndmask_b32 v24, v29, v28, s[44:45]\n\t" \
    "v_mov_b32 v25, v24\n\t" \
    "v_mov_b32 v26, v24\n\t" \
    "v_mov_b32 v27, v24\n\t" \
    "s_waitcnt vmcnt(13)\n\t" \
    "s_waitcnt vmcnt(12)\n\t" \
    "s_waitcnt lgkmcnt(0)\n\t" \
    "v_mfma_f32_16x16x32_bf16 v[44:47], v[128:131], v[180:183], v[44:47]\n\t" \
    "ds_read_b128 v[100:103], v3 offset:20480\n\t" \
    "v_mfma_f32_16x16x32_bf16 v[72:75], v[128:131], v[184:187], v[72:75]\n\t" \
    "ds_read_b128 v[104:107], v3 offset:21504\n\t" \
    "v_mfma_f32_16x16x32_bf16 v[48:51], v[132:135], v[180:183], v[48:51]\n\t" \
    "ds_read_b128 v[108:111], v3 offset:22528\n\t" \
    "v_mfma_f32_16x16x32_bf16 v[76:79], v[132:135], v[184:187], v[76:79]\n\t" \
    "ds_read_b128 v[112:115], v3 offset:23552\n\t" \
    "v_mfma_f32_16x16x32_bf16 v[52:55], v[136:139], v[180:183], v[52:55]\n\t" \
    "ds_read_b128 v[116:119], v3 offset:24576\n\t" \
    "v_mfma_f32_16x16x32_bf16 v[80:83], v[136:139], v[184:187], v[80:83]\n\t" \
    "ds_read_b128 v[120:123], v3 offset:25600\n\t" \
    "v_mfma_f32_16x16x32_bf16 v[56:59], v[140:143], v[180:183], v[56:59]\n\t" \
    "ds_read_b128 v[124:127], v3 offset:26624\n\t" \
    "v_mfma_f32_16x16x32_bf16 v[84:87], v[140:143], v[184:187], v[84:87]\n\t" \
    "v_mfma_f32_16x16x32_bf16 v[60:63], v[180:183], v[144:147], v[60:63]\n\t" \
    "v_mfma_f32_16x16x32_bf16 v[88:91], v[184:187], v[144:147], v[88:91]\n\t" \
    "v_mfma_f32_16x16x32_bf16 v[64:67], v[180:183], v[148:151], v[64:67]\n\t" \
    "v_mfma_f32_16x16x32_bf16 v[92:95], v[184:187], v[148:151], v[92:95]\n\t" \
    "v_mfma_f32_16x16x32_bf16 v[68:71], v[180:183], v[152:155], v[68:71]\n\t" \
    "v_mfma_f32_16x16x32_bf16 v[96:99], v[184:187], v[152:155], v[96:99]\n\t" \
    "s_waitcnt vmcnt(11)\n\t" \
    "s_waitcnt vmcnt(10)\n\t" \
    "s_waitcnt lgkmcnt(0)\n\t" \
    "v_mfma_f32_16x16x32_bf16 v[44:47], v[100:103], v[188:191], v[44:47]\n\t" \
    "ds_read_b128 v[128:131], v3 offset:27648\n\t" \
    "v_mfma_f32_16x16x32_bf16 v[72:75], v[100:103], v[192:195], v[72:75]\n\t" \
    "ds_read_b128 v[132:135], v3 offset:28672\n\t" \
    "v_mfma_f32_16x16x32_bf16 v[48:51], v[104:107], v[188:191], v[48:51]\n\t" \
    "ds_read_b128 v[136:139], v3 offset:29696\n\t" \
    "v_mfma_f32_16x16x32_bf16 v[76:79], v[104:107], v[192:195], v[76:79]\n\t" \
    "ds_read_b128 v[140:143], v3 offset:30720\n\t" \
    "v_mfma_f32_16x16x32_bf16 v[52:55], v[108:111], v[188:191], v[52:55]\n\t" \
    "ds_read_b128 v[144:147], v3 offset:31744\n\t" \
    "v_mfma_f32_16x16x32_bf16 v[80:83], v[108:111], v[192:195], v[80:83]\n\t" \
    "ds_read_b128 v[148:151], v3 offset:32768\n\t" \
    "v_mfma_f32_16x16x32_bf16 v[56:59], v[112:115], v[188:191], v[56:59]\n\t" \
    "ds_read_b128 v[152:155], v3 offset:33792\n\t" \
    "v_mfma_f32_16x16x32_bf16 v[84:87], v[112:115], v[192:195], v[84:87]\n\t" \
    "v_mfma_f32_16x16x32_bf16 v[60:63], v[188:191], v[116:119], v[60:63]\n\t" \
    "v_mfma_f32_16x16x32_bf16 v[88:91], v[192:195], v[116:119], v[88:91]\n\t" \
    "v_mfma_f32_16x16x32_bf16 v[64:67], v[188:191], v[120:123], v[64:67]\n\t" \
    "v_mfma_f32_16x16x32_bf16 v[92:95], v[192:195], v[120:123], v[92:95]\n\t" \
    "v_mfma_f32_16x16x32_bf16 v[68:71], v[188:191], v[124:127], v[68:71]\n\t" \
    "v_mfma_f32_16x16x32_bf16 v[96:99], v[192:195], v[124:127], v[96:99]\n\t" \
    "s_waitcnt vmcnt(9)\n\t" \
    "s_waitcnt vmcnt(8)\n\t" \
    "s_waitcnt lgkmcnt(0)\n\t" \
    "v_mfma_f32_16x16x32_bf16 v[44:47], v[128:131], v[196:199], v[44:47]\n\t" \
    "ds_read_b128 v[100:103], v3 offset:34816\n\t" \
    "v_mfma_f32_16x16x32_bf16 v[72:75], v[128:131], v[200:203], v[72:75]\n\t" \
    "ds_read_b128 v[104:107], v3 offset:35840\n\t" \
    "v_mfma_f32_16x16x32_bf16 v[48:51], v[132:135], v[196:199], v[48:51]\n\t" \
    "ds_read_b128 v[108:111], v3 offset:36864\n\t" \
    "v_mfma_f32_16x16x32_bf16 v[76:79], v[132:135], v[200:203], v[76:79]\n\t" \
    "ds_read_b128 v[112:115], v3 offset:37888\n\t" \
    "v_mfma_f32_16x16x32_bf16 v[52:55], v[136:139], v[196:199], v[52:55]\n\t" \
    "ds_read_b128 v[116:119], v3 offset:38912\n\t" \
    "v_mfma_f32_16x16x32_bf16 v[80:83], v[136:139], v[200:203], v[80:83]\n\t" \
    "ds_read_b128 v[120:123], v3 offset:39936\n\t" \
    "v_mfma_f32_16x16x32_bf16 v[56:59], v[140:143], v[196:199], v[56:59]\n\t" \
    "ds_read_b128 v[124:127], v3 offset:40960\n\t" \
    "v_mfma_f32_16x16x32_bf16 v[84:87], v[140:143], v[200:203], v[84:87]\n\t" \
    "v_mfma_f32_16x16x32_bf16 v[60:63], v[196:199], v[144:147], v[60:63]\n\t" \
    "v_mfma_f32_16x16x32_bf16 v[88:91], v[200:203], v[144:147], v[88:91]\n\t" \
    "v_mfma_f32_16x16x32_bf16 v[64:67], v[196:199], v[148:151], v[64:67]\n\t" \
    "v_mfma_f32_16x16x32_bf16 v[92:95], v[200:203], v[148:151], v[92:95]\n\t" \
    "v_mfma_f32_16x16x32_bf16 v[68:71], v[196:199], v[152:155], v[68:71]\n\t" \
    "v_mfma_f32_16x16x32_bf16 v[96:99], v[200:203], v[152:155], v[96:99]\n\t" \
    "s_waitcnt vmcnt(7)\n\t" \
    "s_waitcnt vmcnt(6)\n\t" \
    "s_waitcnt lgkmcnt(0)\n\t" \
    "v_mfma_f32_16x16x32_bf16 v[44:47], v[100:103], v[204:207], v[44:47]\n\t" \
    "ds_read_b128 v[128:131], v3 offset:41984\n\t" \
    "v_mfma_f32_16x16x32_bf16 v[72:75], v[100:103], v[208:211], v[72:75]\n\t" \
    "ds_read_b128 v[132:135], v3 offset:43008\n\t" \
    "v_mfma_f32_16x16x32_bf16 v[48:51], v[104:107], v[204:207], v[48:51]\n\t" \
    "ds_read_b128 v[136:139], v3 offset:44032\n\t" \
    "v_mfma_f32_16x16x32_bf16 v[76:79], v[104:107], v[208:211], v[76:79]\n\t" \
    "ds_read_b128 v[140:143], v3 offset:45056\n\t" \
    "v_mfma_f32_16x16x32_bf16 v[52:55], v[108:111], v[204:207], v[52:55]\n\t" \
    "ds_read_b128 v[144:147], v3 offset:46080\n\t" \
    "v_mfma_f32_16x16x32_bf16 v[80:83], v[108:111], v[208:211], v[80:83]\n\t" \
    "ds_read_b128 v[148:151], v3 offset:47104\n\t" \
    "v_mfma_f32_16x16x32_bf16 v[56:59], v[112:115], v[204:207], v[56:59]\n\t" \
    "ds_read_b128 v[152:155], v3 offset:48128\n\t" \
    "v_mfma_f32_16x16x32_bf16 v[84:87], v[112:115], v[208:211], v[84:87]\n\t" \
    "v_mfma_f32_16x16x32_bf16 v[60:63], v[204:207], v[116:119], v[60:63]\n\t" \
    "v_mfma_f32_16x16x32_bf16 v[88:91], v[208:211], v[116:119], v[88:91]\n\t" \
    "v_mfma_f32_16x16x32_bf16 v[64:67], v[204:207], v[120:123], v[64:67]\n\t" \
    "v_mfma_f32_16x16x32_bf16 v[92:95], v[208:211], v[120:123], v[92:95]\n\t" \
    "v_mfma_f32_16x16x32_bf16 v[68:71], v[204:207], v[124:127], v[68:71]\n\t" \
    "v_mfma_f32_16x16x32_bf16 v[96:99], v[208:211], v[124:127], v[96:99]\n\t" \
    "s_waitcnt vmcnt(5)\n\t" \
    "s_waitcnt vmcnt(4)\n\t" \
    "s_waitcnt lgkmcnt(0)\n\t" \
    "v_mfma_f32_16x16x32_bf16 v[44:47], v[128:131], v[212:215], v[44:47]\n\t" \
    "ds_read_b128 v[100:103], v3 offset:49152\n\t" \
    "v_mfma_f32_16x16x32_bf16 v[72:75], v[128:131], v[216:219], v[72:75]\n\t" \
    "ds_read_b128 v[104:107], v3 offset:50176\n\t" \
    "v_mfma_f32_16x16x32_bf16 v[48:51], v[132:135], v[212:215], v[48:51]\n\t" \
    "ds_read_b128 v[108:111], v3 offset:51200\n\t" \
    "v_mfma_f32_16x16x32_bf16 v[76:79], v[132:135], v[216:219], v[76:79]\n\t" \
    "ds_read_b128 v[112:115], v3 offset:52224\n\t" \
    "v_mfma_f32_16x16x32_bf16 v[52:55], v[136:139], v[212:215], v[52:55]\n\t" \
    "ds_read_b128 v[116:119], v3 offset:53248\n\t" \
    "v_mfma_f32_16x16x32_bf16 v[80:83], v[136:139], v[216:219], v[80:83]\n\t" \
    "ds_read_b128 v[120:123], v3 offset:54272\n\t" \
    "v_mfma_f32_16x16x32_bf16 v[56:59], v[140:143], v[212:215], v[56:59]\n\t" \
    "ds_read_b128 v[124:127], v3 offset:55296\n\t" \
    "v_mfma_f32_16x16x32_bf16 v[84:87], v[140:143], v[216:219], v[84:87]\n\t" \
    "v_mfma_f32_16x16x32_bf16 v[60:63], v[212:215], v[144:147], v[60:63]\n\t" \
    "v_mfma_f32_16x16x32_bf16 v[88:91], v[216:219], v[144:147], v[88:91]\n\t" \
    "v_mfma_f32_16x16x32_bf16 v[64:67], v[212:215], v[148:151], v[64:67]\n\t" \
    "v_mfma_f32_16x16x32_bf16 v[92:95], v[216:219], v[148:151], v[92:95]\n\t" \
    "v_mfma_f32_16x16x32_bf16 v[68:71], v[212:215], v[152:155], v[68:71]\n\t" \
    "v_mfma_f32_16x16x32_bf16 v[96:99], v[216:219], v[152:155], v[96:99]\n\t" \
    "s_waitcnt vmcnt(3)\n\t" \
    "s_waitcnt vmcnt(2)\n\t" \
    "s_waitcnt lgkmcnt(0)\n\t" \
    "v_mfma_f32_16x16x32_bf16 v[44:47], v[100:103], v[156:159], v[44:47]\n\t" \
    "ds_read_b128 v[128:131], v3 offset:56320\n\t" \
    "v_mfma_f32_16x16x32_bf16 v[72:75], v[100:103], v[160:163], v[72:75]\n\t" \
    "ds_read_b128 v[132:135], v3 offset:57344\n\t" \
    "v_mfma_f32_16x16x32_bf16 v[48:51], v[104:107], v[156:159], v[48:51]\n\t" \
    "ds_read_b128 v[136:139], v3 offset:58368\n\t" \
    "v_mfma_f32_16x16x32_bf16 v[76:79], v[104:107], v[160:163], v[76:79]\n\t" \
    "ds_read_b128 v[140:143], v3 offset:59392\n\t" \
    "v_mfma_f32_16x16x32_bf16 v[52:55], v[108:111], v[156:159], v[52:55]\n\t" \
    "ds_read_b128 v[144:147], v3 offset:60416\n\t" \
    "v_mfma_f32_16x16x32_bf16 v[80:83], v[108:111], v[160:163], v[80:83]\n\t" \
    "ds_read_b128 v[148:151], v3 offset:61440\n\t" \
    "v_mfma_f32_16x16x32_bf16 v[56:59], v[112:115], v[156:159], v[56:59]\n\t" \
    "ds_read_b128 v[152:155], v3 offset:62464\n\t" \
    "v_mfma_f32_16x16x32_bf16 v[84:87], v[112:115], v[160:163], v[84:87]\n\t" \
    "v_mfma_f32_16x16x32_bf16 v[60:63], v[156:159], v[116:119], v[60:63]\n\t" \
    "v_mfma_f32_16x16x32_bf16 v[88:91], v[160:163], v[116:119], v[88:91]\n\t" \
    "v_mfma_f32_16x16x32_bf16 v[64:67], v[156:159], v[120:123], v[64:67]\n\t" \
    "v_mfma_f32_16x16x32_bf16 v[92:95], v[160:163], v[120:123], v[92:95]\n\t" \
    "v_mfma_f32_16x16x32_bf16 v[68:71], v[156:159], v[124:127], v[68:71]\n\t" \
    "v_mfma_f32_16x16x32_bf16 v[96:99], v[160:163], v[124:127], v[96:99]\n\t" \
    "s_waitcnt vmcnt(1)\n\t" \
    "s_waitcnt vmcnt(0)\n\t" \
    "s_waitcnt lgkmcnt(0)\n\t" \
    "v_mfma_f32_16x16x32_bf16 v[44:47], v[128:131], v[164:167], v[44:47]\n\t" \
    "ds_read_b128 v[100:103], v3 offset:63488\n\t" \
    "v_mfma_f32_16x16x32_bf16 v[72:75], v[128:131], v[168:171], v[72:75]\n\t" \
    "ds_read_b128 v[104:107], v3 offset:64512\n\t" \
    "v_mfma_f32_16x16x32_bf16 v[48:51], v[132:135], v[164:167], v[48:51]\n\t" \
    "ds_read_b128 v[108:111], v4 offset:0\n\t" \
    "v_mfma_f32_16x16x32_bf16 v[76:79], v[132:135], v[168:171], v[76:79]\n\t" \
    "ds_read_b128 v[112:115], v4 offset:1024\n\t" \
    "v_mfma_f32_16x16x32_bf16 v[52:55], v[136:139], v[164:167], v[52:55]\n\t" \
    "ds_read_b128 v[116:119], v4 offset:2048\n\t" \
    "v_mfma_f32_16x16x32_bf16 v[80:83], v[136:139], v[168:171], v[80:83]\n\t" \
    "ds_read_b128 v[120:123], v4 offset:3072\n\t" \
    "v_mfma_f32_16x16x32_bf16 v[56:59], v[140:143], v[164:167], v[56:59]\n\t" \
    "ds_read_b128 v[124:127], v4 offset:4096\n\t" \
    "v_mfma_f32_16x16x32_bf16 v[84:87], v[140:143], v[168:171], v[84:87]\n\t" \
    "v_mfma_f32_16x16x32_bf16 v[60:63], v[164:167], v[144:147], v[60:63]\n\t" \
    "v_mfma_f32_16x16x32_bf16 v[88:91], v[168:171], v[144:147], v[88:91]\n\t" \
    "v_mfma_f32_16x16x32_bf16 v[64:67], v[164:167], v[148:151], v[64:67]\n\t" \
    "v_mfma_f32_16x16x32_bf16 v[92:95], v[168:171], v[148:151], v[92:95]\n\t" \
    "v_mfma_f32_16x16x32_bf16 v[68:71], v[164:167], v[152:155], v[68:71]\n\t" \
    "v_mfma_f32_16x16x32_bf16 v[96:99], v[168:171], v[152:155], v[96:99]\n\t" \
    "s_waitcnt lgkmcnt(0)\n\t" \
    "v_mfma_f32_16x16x32_bf16 v[44:47], v[100:103], v[20:23], v[44:47]\n\t" \
    "v_mfma_f32_16x16x32_bf16 v[72:75], v[100:103], v[24:27], v[72:75]\n\t" \
    "v_mfma_f32_16x16x32_bf16 v[48:51], v[104:107], v[20:23], v[48:51]\n\t" \
    "v_mfma_f32_16x16x32_bf16 v[76:79], v[104:107], v[24:27], v[76:79]\n\t" \
    "v_mfma_f32_16x16x32_bf16 v[52:55], v[108:111], v[20:23], v[52:55]\n\t" \
    "v_mfma_f32_16x16x32_bf16 v[80:83], v[108:111], v[24:27], v[80:83]\n\t" \
    "v_mfma_f32_16x16x32_bf16 v[56:59], v[112:115], v[20:23], v[56:59]\n\t" \
    "v_mfma_f32_16x16x32_bf16 v[84:87], v[112:115], v[24:27], v[84:87]\n\t" \
    "v_mfma_f32_16x16x32_bf16 v[60:63], v[20:23], v[116:119], v[60:63]\n\t" \
    "v_mfma_f32_16x16x32_bf16 v[88:91], v[24:27], v[116:119], v[88:91]\n\t" \
    "v_mfma_f32_16x16x32_bf16 v[64:67], v[20:23], v[120:123], v[64:67]\n\t" \
    "v_mfma_f32_16x16x32_bf16 v[92:95], v[24:27], v[120:123], v[92:95]\n\t" \
    "v_mfma_f32_16x16x32_bf16 v[68:71], v[20:23], v[124:127], v[68:71]\n\t" \
    "v_mfma_f32_16x16x32_bf16 v[96:99], v[24:27], v[124:127], v[96:99]\n\t" \
    "s_nop 7\n\t" \
    "s_nop 1\n\t"

#define VCLOB10(a) "v" #a "0","v" #a "1","v" #a "2","v" #a "3","v" #a "4","v" #a "5","v" #a "6","v" #a "7","v" #a "8","v" #a "9"
#define ACLOB10(a) "a" #a "0","a" #a "1","a" #a "2","a" #a "3","a" #a "4","a" #a "5","a" #a "6","a" #a "7","a" #a "8","a" #a "9"
#define QKV_CLOB "memory", "scc", "s44", "s45", "s46", "v237",  "v18","v19", VCLOB10(2), VCLOB10(3), "v40","v41","v42","v43", \
    VCLOB10(10),VCLOB10(11),VCLOB10(12),VCLOB10(13),VCLOB10(14),VCLOB10(15),VCLOB10(16),VCLOB10(17), \
    VCLOB10(18),VCLOB10(19),VCLOB10(20),VCLOB10(21)
#define QKV_OUTS "={v[44:47]}"(acc[0][0]), "={v[48:51]}"(acc[0][1]), "={v[52:55]}"(acc[0][2]), "={v[56:59]}"(acc[0][3]), "={v[60:63]}"(acc[0][4]), \
    "={v[64:67]}"(acc[0][5]), "={v[68:71]}"(acc[0][6]), "={v[72:75]}"(acc[1][0]), "={v[76:79]}"(acc[1][1]), "={v[80:83]}"(acc[1][2]), \
    "={v[84:87]}"(acc[1][3]), "={v[88:91]}"(acc[1][4]), "={v[92:95]}"(acc[1][5]), "={v[96:99]}"(acc[1][6]), \
    "={v8}"(rs[0]), "={v9}"(rs[1]), "={v[10:13]}"(r0), "={v[14:17]}"(r1), \
    "+{v0}"(vg0), "+{v1}"(vg1)
#define QKV_INS "{v2}"(l0), "{v3}"(l0 + 65536), "{v4}"(l0 + 131072), "{v5}"(vtb), "{v6}"(vtn), "{v7}"(vex), \
    "{v236}"(vpc), [wb] "s"(wbase), [sg] "s"(gsrc), [ss] "s"(ssrc)

template <bool IS_A>
DI void qkv_phase(char* lds, const char* XT, const char* stats, const char* WT, char* QM, char* QX, char* IMG, float* KVB,
                  unsigned* dep, unsigned* cnt, unsigned id, unsigned wbase) {
    int tid = threadIdx.x, bid = blockIdx.x;
    asm volatile("" : "+v"(tid)); asm volatile("" : "+s"(bid));
    const int lane = tid & 63, w = __builtin_amdgcn_readfirstlane(tid >> 6);
    const int r16 = lane & 15, g = lane >> 4;
    const int m = w & 3, role = w >> 2;
    int mt, nt;
    { const int b = bid, xcd = b & 7, i = b >> 3; mt = (xcd >> 1) * 4 + (i & 3); nt = (xcd & 1) * 8 + (i >> 2); }
    const int srow0 = mt * 128 + m * 32;

    f32x4 acc[2][7], r0, r1;
    float rs[2];
    {
        const unsigned lb = (unsigned)(uintptr_t)lds;
        const unsigned vl16 = lane * 16;
        dma_loop(WT + (size_t)nt * WT_NT + w * 1024, lb + w * 1024, 9, 8192, vl16);
        const unsigned vpc = lb + LDS_FLAG + 16;
        if (role) {
            dma_loop(WT + (size_t)nt * WT_NT + (72 + m) * 1024, lb + (72 + m) * 1024, 6, 4096, vl16);
            asm volatile("s_waitcnt vmcnt(6)\n\tds_add_u32 %0, %1" :: "v"(vpc), "v"(1u) : "memory");
            dma_loop(WT + (size_t)nt * WT_NT + (96 + m) * 1024, lb + (96 + m) * 1024, 10, 4096, vl16);
        }
        const char* gsrc = XT + (size_t)(mt * 8 + 2 * m) * (18 * 1024) + 4096;
        const char* ssrc = stats + (size_t)srow0 * 64;
        unsigned vg0 = lane * 16, vg1 = role ? r16 * 64 + g * 16 : lane * 16 + 18 * 1024;
        const unsigned l0 = lb + lane * 16;
        const unsigned vtb = lb + 136 * 1024 + (m * 32 + r16) * 4, vtn = lb + 136 * 1024 + (m * 32 + 4 * g) * 4;
        const unsigned vex = lb + m * 4096 + lane * 16;
        if (dep) wait_dep(dep + mt * 64, 16, lds, id, w);
        if (role) {
            if constexpr (IS_A) asm volatile(QKV_ASM_SA : QKV_OUTS : QKV_INS : QKV_CLOB);
            else                asm volatile(QKV_ASM_SB : QKV_OUTS : QKV_INS : QKV_CLOB);
        } else {
            if constexpr (IS_A) asm volatile(QKV_ASM_CA : QKV_OUTS : QKV_INS : QKV_CLOB);
            else                asm volatile(QKV_ASM_CB : QKV_OUTS : QKV_INS : QKV_CLOB);
        }
    }

    const int ha = 2 * nt;
    const bool odd = r16 & 1;
    constexpr float L2E = 1.4426950408889634f;
    if constexpr (!IS_A) {
        constexpr float QS = 0.23570226039551584f * L2E;
        const int kt = srow0 >> 5;
        char* img0 = IMG + (size_t)ha * IMG_HEAD + (size_t)(kt >> 2) * IMG_GROUP;
        if (!role) {
            const unsigned hq = g >> 1, hh = g & 1;
            char* qb = QM + ((size_t)ha * 64 + kt) * 1024;
            char* kb = img0 + (kt & 3) * 1024;
            const unsigned qo = hq * 65536 + hh * 512 + r16 * 16;
            const unsigned ko = hq * (unsigned)IMG_HEAD + hh * 512 + swap34(r16) * 16;
#pragma unroll
            for (int a = 0; a < 2; ++a) {
                const float rq = rs[a] * QS, rk = rs[a];
                const f32x4 q0 = acc[a][0] * rq, q1 = acc[a][1] * rq, k0 = acc[a][2] * rk, k1 = acc[a][3] * rk;
                st16(qb + qo + a * 256, (u32x4){pk2(q0[0], q0[1]), pk2(q0[2], q0[3]), pk2(q1[0], q1[1]), pk2(q1[2], q1[3])});
                st16(kb + ko + a * 128, (u32x4){pk2(k0[0], k0[1]), pk2(k0[2], k0[3]), pk2(k1[0], k1[1]), pk2(k1[2], k1[3])});
            }
            char* vb = img0 + 4608 + (kt & 3) * VT_TILE;
            const unsigned vo = g * VT_CHUNK;
#pragma unroll
            for (int c = 0; c < 2; ++c) {
                const f32x4 v0 = acc[0][4 + c] * r0, v1 = acc[1][4 + c] * r1;
                st16(vb + (size_t)c * IMG_HEAD + vo + vpos(r16) * 16, (u32x4){pk2(v0[0], v0[1]), pk2(v0[2], v0[3]), pk2(v1[0], v1[1]), pk2(v1[2], v1[3])});
            }
            const float sc = r16 < 4 ? QS : 1.0f;
            const float cst = (r16 >> 1) == 6 ? 1.0f : 0.0f;
            const f32x4 v0 = acc[0][6] * r0 * sc + cst, v1 = acc[1][6] * r1 * sc + cst;
            u32x4 d;
#pragma unroll
            for (int i = 0; i < 4; ++i) {
                const float rcv = swap_nb(odd ? v0[i] : v1[i]);
                d[i] = odd ? pk2(rcv, v1[i]) : pk2(v0[i], rcv);
            }
            if (r16 >= 8) d = (u32x4){pk2(v0[0], v0[1]), pk2(v0[2], v0[3]), pk2(v1[0], v1[1]), pk2(v1[2], v1[3])};
            const unsigned ao = odd ? 16 : 0;
            const unsigned hx = r16 < 12 ? (r16 >> 1) & 1 : r16 & 1;
            const unsigned vrow = r16 < 12 ? 16 + (r16 & 1) : (r16 < 14 ? VT_ONES : VT_ZERO);
            const unsigned oimg = (unsigned)(img0 - IMG) + hx * (unsigned)IMG_HEAD;
            const int oq = (int)(QX - IMG) + (int)(((ha + hx) * S_ + srow0 + ao + 4 * g) * 4);
            const int ok = (int)(oimg + 4096 + (kt & 3) * 128 + swap34(ao + 4 * g) * 4);
            const int ov = (int)(oimg + 4608 + (kt & 3) * VT_TILE + vo + vrow * 16);
            st16(IMG + (r16 < 4 ? oq : (r16 < 8 ? ok : ov)), d);
        }
        drain_and_sync();
        if (tid == 0) cnt_add(cnt + (C_KV + nt) * 64);
    } else {
        const int st0 = srow0 >> 4;
        f32x4* lr = (f32x4*)(lds + 57344);
        if (role) {
#pragma unroll
            for (int a = 0; a < 2; ++a) {
                const float r = rs[a], rl = r * L2E;
                const f32x4 q0 = acc[a][0], q1 = acc[a][1];
                st16(QM + ((size_t)(st0 + a) * 32 + ha) * 512 + lane * 16,
                    (u32x4){pk2(phi_s(q0[0], r, rl), phi_s(q0[1], r, rl)), pk2(phi_s(q0[2], r, rl), phi_s(q0[3], r, rl)),
                            pk2(phi_s(q1[0], r, rl), phi_s(q1[1], r, rl)), pk2(phi_s(q1[2], r, rl), phi_s(q1[3], r, rl))});
            }
        } else {
            const f32x4 l0 = r0 * L2E, l1 = r1 * L2E;
            bf16x8 PK[2], VV[2], EX;
#pragma unroll
            for (int c = 0; c < 2; ++c) {
                const f32x4 k0 = acc[0][2 + c], k1 = acc[1][2 + c];
                PK[c] = asbf8((u32x4){pk2(phi_s(k0[0], r0[0], l0[0]), phi_s(k0[1], r0[1], l0[1])), pk2(phi_s(k0[2], r0[2], l0[2]), phi_s(k0[3], r0[3], l0[3])),
                                      pk2(phi_s(k1[0], r1[0], l1[0]), phi_s(k1[1], r1[1], l1[1])), pk2(phi_s(k1[2], r1[2], l1[2]), phi_s(k1[3], r1[3], l1[3]))});
                const f32x4 v0 = acc[0][4 + c] * r0, v1 = acc[1][4 + c] * r1;
                VV[c] = asbf8((u32x4){pk2(v0[0], v0[1]), pk2(v0[2], v0[3]), pk2(v1[0], v1[1]), pk2(v1[2], v1[3])});
            }
            f32x4 v0, v1;
            const float cst = r16 == 12 ? 1.0f : 0.0f;
#pragma unroll
            for (int i = 0; i < 4; ++i) {
                const float p0 = phi_s(acc[0][6][i], r0[i], l0[i]), p1 = phi_s(acc[1][6][i], r1[i], l1[i]);
                v0[i] = r16 < 8 ? p0 : fmaf(acc[0][6][i], r0[i], cst);
                v1[i] = r16 < 8 ? p1 : fmaf(acc[1][6][i], r1[i], cst);
            }
            EX = asbf8((u32x4){pk2(v0[0], v0[1]), pk2(v0[2], v0[3]), pk2(v1[0], v1[1]), pk2(v1[2], v1[3])});
            const f32x4 z4 = {0.f, 0.f, 0.f, 0.f};
            f32x4 dt[7];
            dt[0] = mfma16(VV[0], PK[0], z4);
            dt[1] = mfma16(VV[1], PK[1], z4);
            dt[2] = mfma16(VV[0], EX, z4);
            dt[3] = mfma16(VV[1], EX, z4);
            dt[4] = mfma16(EX, PK[0], z4);
            dt[5] = mfma16(EX, PK[1], z4);
            dt[6] = mfma16(EX, EX, z4);
            u32x4 d;
#pragma unroll
            for (int i = 0; i < 4; ++i) {
                const float rcv = swap_nb(odd ? v0[i] : v1[i]);
                d[i] = odd ? pk2(rcv, v1[i]) : pk2(v0[i], rcv);
            }
            if (r16 < 4) st16(QX + (size_t)(st0 + (odd ? 1 : 0)) * PX_ST + (ha + (r16 >> 1)) * 64 + 16 * g, d);
#pragma unroll
            for (int t = 0; t < 7; ++t) lr[(m * 7 + t) * 64 + lane] = dt[t];
        }
        __syncthreads();
        if (w < 7) {
            const int t = w;
            const f32x4 s = (lr[t * 64 + lane] + lr[(7 + t) * 64 + lane]) + (lr[(14 + t) * 64 + lane] + lr[(21 + t) * 64 + lane]);
            const bool excol = (t == 2 || t == 3 || t == 6);
            const int hb = (t == 6) ? ((r16 >> 1) & 1) : (t & 1);
            const bool colok = !excol || ((r16 >> 1) == 2 + hb);
            const int d = excol ? 16 + (r16 & 1) : r16;
            float* base = KVB + (ha + hb) * (19 * 20) + d;
#pragma unroll
            for (int i = 0; i < 4; ++i) {
                const int e = (t < 4) ? 4 * g + i : ((g == 3) ? 18 : 16 + (i & 1));
                const bool ok = colok && (t < 4 || (g == 2 && (i >> 1) == hb) || (g == 3 && i == 0));
                if (ok) atomicAdd(base + e * 20, s[i]);
            }
        }
        drain_and_sync();
        if (tid == 0) cnt_add(cnt + (C_AG + mt) * 64);
    }
}

#define OPROJ_ASM_WA \
    "s_mov_b32 s42, m0\n\t" \
    "s_mov_b32 m0, %[lb]\n\t" \
    "s_nop 0\n\t" \
    "global_load_dwordx4 v[40:43], v3, %[sx] offset:0 sc1\n\t" \
    "global_load_dwordx4 v[44:47], v3, %[sx] offset:1024 sc1\n\t" \
    "global_load_dwordx2 v[48:49], v4, %[sx] sc1\n\t" \
    "global_load_lds_dwordx4 v0, s[40:41]\n\t" \
    "s_add_u32 m0, m0, 8192\n\t" \
    "s_add_u32 s40, s40, 8192\n\t" \
    "s_addc_u32 s41, s41, 0\n\t" \
    "global_load_lds_dwordx4 v0, s[40:41]\n\t" \
    "s_add_u32 m0, m0, 8192\n\t" \
    "s_add_u32 s40, s40, 8192\n\t" \
    "s_addc_u32 s41, s41, 0\n\t" \
    "global_load_lds_dwordx4 v0, s[40:41]\n\t" \
    "s_add_u32 m0, m0, 8192\n\t" \
    "s_add_u32 s40, s40, 8192\n\t" \
    "s_addc_u32 s41, s41, 0\n\t" \
    "global_load_lds_dwordx4 v0, s[40:41]\n\t" \
    "s_add_u32 m0, m0, 8192\n\t" \
    "s_add_u32 s40, s40, 8192\n\t" \
    "s_addc_u32 s41, s41, 0\n\t" \
    "global_load_lds_dwordx4 v0, s[40:41]\n\t" \
    "s_add_u32 m0, m0, 8192\n\t" \
    "s_add_u32 s40, s40, 8192\n\t" \
    "s_addc_u32 s41, s41, 0\n\t" \
    "global_load_lds_dwordx4 v0, s[40:41]\n\t" \
    "s_add_u32 m0, m0, 8192\n\t" \
    "s_add_u32 s40, s40, 8192\n\t" \
    "s_addc_u32 s41, s41, 0\n\t" \
    "global_load_lds_dwordx4 v0, s[40:41]\n\t" \
    "s_add_u32 m0, m0, 8192\n\t" \
    "s_add_u32 s40, s40, 8192\n\t" \
    "s_addc_u32 s41, s41, 0\n\t" \
    "global_load_lds_dwordx4 v0, s[40:41]\n\t" \
    "s_add_u32 m0, m0, 8192\n\t" \
    "s_add_u32 s40, s40, 8192\n\t" \
    "s_addc_u32 s41, s41, 0\n\t" \
    "global_load_lds_dwordx4 v0, s[40:41]\n\t" \
    "s_add_u32 m0, m0, 8192\n\t" \
    "s_add_u32 s40, s40, 8192\n\t" \
    "s_addc_u32 s41, s41, 0\n\t" \
    "global_load_lds_dwordx4 v0, s[40:41]\n\t" \
    "s_add_u32 m0, m0, %[dl]\n\t" \
    "s_add_u32 s40, s40, %[dl]\n\t" \
    "s_addc_u32 s41, s41, 0\n\t" \
    "global_load_lds_dwordx4 v0, s[40:41]\n\t" \
    "s_mov_b32 m0, s42\n\t" \
    "v_mov_b32 v20, 0\n\t" \
    "v_mov_b32 v21, 0\n\t" \
    "v_mov_b32 v22, 0\n\t" \
    "v_mov_b32 v23, 0\n\t" \
    "ds_write_b128 v12, v[20:23] offset:0\n\t" \
    "ds_write_b128 v12, v[20:23] offset:1024\n\t" \
    "ds_write_b128 v12, v[20:23] offset:2048\n\t" \
    "ds_write_b128 v12, v[20:23] offset:3072\n\t" \
    "s_mov_b32 exec_hi, 0xffff\n\t" \
    "ds_write_b128 v12, v[20:23] offset:4096\n\t" \
    "s_mov_b32 exec_hi, -1\n\t"
#define OPROJ_ASM_WB \
    "s_mov_b32 s42, m0\n\t" \
    "s_mov_b32 m0, %[lb]\n\t" \
    "s_nop 0\n\t" \
    "global_load_dwordx4 v[40:43], v3, %[sx] offset:0 sc1\n\t" \
    "global_load_dwordx4 v[44:47], v3, %[sx] offset:1024 sc1\n\t" \
    "global_load_dwordx2 v[48:49], v4, %[sx] sc1\n\t" \
    "global_load_lds_dwordx4 v0, s[40:41]\n\t" \
    "s_add_u32 m0, m0, 8192\n\t" \
    "s_add_u32 s40, s40, 8192\n\t" \
    "s_addc_u32 s41, s41, 0\n\t" \
    "global_load_lds_dwordx4 v0, s[40:41]\n\t" \
    "s_add_u32 m0, m0, 8192\n\t" \
    "s_add_u32 s40, s40, 8192\n\t" \
    "s_addc_u32 s41, s41, 0\n\t" \
    "global_load_lds_dwordx4 v0, s[40:41]\n\t" \
    "s_add_u32 m0, m0, 8192\n\t" \
    "s_add_u32 s40, s40, 8192\n\t" \
    "s_addc_u32 s41, s41, 0\n\t" \
    "global_load_lds_dwordx4 v0, s[40:41]\n\t" \
    "s_add_u32 m0, m0, 8192\n\t" \
    "s_add_u32 s40, s40, 8192\n\t" \
    "s_addc_u32 s41, s41, 0\n\t" \
    "global_load_lds_dwordx4 v0, s[40:41]\n\t" \
    "s_add_u32 m0, m0, 8192\n\t" \
    "s_add_u32 s40, s40, 8192\n\t" \
    "s_addc_u32 s41, s41, 0\n\t" \
    "global_load_lds_dwordx4 v0, s[40:41]\n\t" \
    "s_add_u32 m0, m0, 8192\n\t" \
    "s_add_u32 s40, s40, 8192\n\t" \
    "s_addc_u32 s41, s41, 0\n\t" \
    "global_load_lds_dwordx4 v0, s[40:41]\n\t" \
    "s_add_u32 m0, m0, 8192\n\t" \
    "s_add_u32 s40, s40, 8192\n\t" \
    "s_addc_u32 s41, s41, 0\n\t" \
    "global_load_lds_dwordx4 v0, s[40:41]\n\t" \
    "s_add_u32 m0, m0, 8192\n\t" \
    "s_add_u32 s40, s40, 8192\n\t" \
    "s_addc_u32 s41, s41, 0\n\t" \
    "global_load_lds_dwordx4 v0, s[40:41]\n\t" \
    "s_add_u32 m0, m0, 8192\n\t" \
    "s_add_u32 s40, s40, 8192\n\t" \
    "s_addc_u32 s41, s41, 0\n\t" \
    "global_load_lds_dwordx4 v0, s[40:41]\n\t" \
    "s_add_u32 m0, m0, %[dl]\n\t" \
    "s_add_u32 s40, s40, %[dl]\n\t" \
    "s_addc_u32 s41, s41, 0\n\t" \
    "global_load_lds_dwordx4 v0, s[40:41]\n\t" \
    "s_mov_b32 m0, s42\n\t"
#define OPROJ_ASM_PA \
    "global_load_dwordx4 v[128:131], v1, %[sa] sc1\n\t" \
    "v_add_u32 v1, v2, v1\n\t" \
    "global_load_dwordx4 v[132:135], v1, %[sa] sc1\n\t" \
    "v_add_u32 v1, v2, v1\n\t" \
    "global_load_dwordx4 v[136:139], v1, %[sa] sc1\n\t" \
    "v_add_u32 v1, v2, v1\n\t" \
    "global_load_dwordx4 v[140:143], v1, %[sa] sc1\n\t" \
    "v_add_u32 v1, v2, v1\n\t" \
    "global_load_dwordx4 v[144:147], v1, %[sa] sc1\n\t" \
    "v_add_u32 v1, v2, v1\n\t" \
    "global_load_dwordx4 v[148:151], v1, %[sa] sc1\n\t" \
    "v_add_u32 v1, v2, v1\n\t" \
    "global_load_dwordx4 v[152:155], v1, %[sa] sc1\n\t" \
    "v_add_u32 v1, v2, v1\n\t" \
    "global_load_dwordx4 v[156:159], v1, %[sa] sc1\n\t" \
    "v_add_u32 v1, v2, v1\n\t" \
    "global_load_dwordx4 v[160:163], v1, %[sa] sc1\n\t" \
    "v_add_u32 v1, v2, v1\n\t" \
    "global_load_dwordx4 v[164:167], v1, %[sa] sc1\n\t" \
    "v_add_u32 v1, v2, v1\n\t" \
    "global_load_dwordx4 v[168:171], v1, %[sa] sc1\n\t" \
    "v_add_u32 v1, v2, v1\n\t" \
    "global_load_dwordx4 v[172:175], v1, %[sa] sc1\n\t" \
    "v_add_u32 v1, v2, v1\n\t" \
    "global_load_dwordx4 v[176:179], v1, %[sa] sc1\n\t" \
    "v_add_u32 v1, v2, v1\n\t" \
    "global_load_dwordx4 v[180:183], v1, %[sa] sc1\n\t" \
    "v_add_u32 v1, v2, v1\n\t" \
    "global_load_dwordx4 v[184:187], v1, %[sa] sc1\n\t" \
    "v_add_u32 v1, v2, v1\n\t" \
    "global_load_dwordx4 v[188:191], v1, %[sa] sc1\n\t" \
    "v_add_u32 v1, v2, v1\n\t"
#define OPROJ_ASM_A \
    "s_cmp_eq_u32 %[kh], 0\n\t" \
    "s_cbranch_scc1 4f\n\t" \
    "s_setprio 2\n\t" \
    "4:\n\t" \
    "global_load_dwordx4 v[192:195], v10, %[skv] offset:0 sc1\n\t" \
    "global_load_dwordx4 v[196:199], v10, %[skv] offset:960 sc1\n\t" \
    "global_load_dwordx4 v[200:203], v10, %[skv] offset:1920 sc1\n\t" \
    "global_load_dwordx4 v[204:207], v10, %[skv] offset:2880 sc1\n\t" \
    "v_add_u32 v10, 3840, v10\n\t" \
    "global_load_dwordx4 v[208:211], v10, %[skv] offset:0 sc1\n\t" \
    "global_load_dwordx4 v[212:215], v10, %[skv] offset:960 sc1\n\t" \
    "global_load_dwordx4 v[216:219], v10, %[skv] offset:1920 sc1\n\t" \
    "v_mov_b32 v52, 0\n\t" \
    "v_mov_b32 v53, 0\n\t" \
    "v_mov_b32 v54, 0\n\t" \
    "v_mov_b32 v55, 0\n\t" \
    "v_mov_b32 v124, 0\n\t" \
    "v_mov_b32 v125, 0\n\t" \
    "v_mov_b32 v126, 0\n\t" \
    "v_mov_b32 v127, 0x358637bd\n\t" \
    "s_waitcnt vmcnt(6)\n\t" \
    "v_cvt_pk_bf16_f32 v192, v192, v193\n\t" \
    "v_cvt_pk_bf16_f32 v193, v194, v195\n\t" \
    "ds_write_b64 v11, v[192:193] offset:0\n\t" \
    "s_waitcnt vmcnt(5)\n\t" \
    "v_cvt_pk_bf16_f32 v196, v196, v197\n\t" \
    "v_cvt_pk_bf16_f32 v197, v198, v199\n\t" \
    "ds_write_b64 v11, v[196:197] offset:768\n\t" \
    "s_waitcnt vmcnt(4)\n\t" \
    "v_cvt_pk_bf16_f32 v200, v200, v201\n\t" \
    "v_cvt_pk_bf16_f32 v201, v202, v203\n\t" \
    "ds_write_b64 v11, v[200:201] offset:1536\n\t" \
    "s_waitcnt vmcnt(3)\n\t" \
    "v_cvt_pk_bf16_f32 v204, v204, v205\n\t" \
    "v_cvt_pk_bf16_f32 v205, v206, v207\n\t" \
    "ds_write_b64 v11, v[204:205] offset:2304\n\t" \
    "s_waitcnt vmcnt(2)\n\t" \
    "v_cvt_pk_bf16_f32 v208, v208, v209\n\t" \
    "v_cvt_pk_bf16_f32 v209, v210, v211\n\t" \
    "ds_write_b64 v11, v[208:209] offset:3072\n\t" \
    "s_waitcnt vmcnt(1)\n\t" \
    "v_cvt_pk_bf16_f32 v212, v212, v213\n\t" \
    "v_cvt_pk_bf16_f32 v213, v214, v215\n\t" \
    "ds_write_b64 v11, v[212:213] offset:3840\n\t" \
    "s_waitcnt vmcnt(0)\n\t" \
    "v_cvt_pk_bf16_f32 v216, v216, v217\n\t" \
    "v_cvt_pk_bf16_f32 v217, v218, v219\n\t" \
    "s_mov_b64 exec, 0xfffff\n\t" \
    "ds_write_b64 v11, v[216:217] offset:4608\n\t" \
    "s_mov_b64 exec, -1\n\t" \
    "s_waitcnt lgkmcnt(0)\n\t" \
    "s_barrier\n\t" \
    "ds_read_b128 v[192:195], v13 offset:0\n\t" \
    "ds_read_b128 v[196:199], v14 offset:0\n\t" \
    "ds_read_b128 v[200:203], v13 offset:1216\n\t" \
    "ds_read_b128 v[204:207], v15 offset:1216\n\t" \
    "ds_read_b128 v[208:211], v13 offset:2432\n\t" \
    "ds_read_b128 v[212:215], v16 offset:2432\n\t" \
    "ds_read_b128 v[216:219], v13 offset:3648\n\t" \
    "ds_read_b128 v[220:223], v17 offset:3648\n\t" \
    "s_waitcnt lgkmcnt(6)\n\t" \
    "v_mfma_f32_16x16x32_bf16 v[224:227], v[192:195], v[128:131], 0\n\t" \
    "v_mfma_f32_16x16x32_bf16 v[228:231], v[196:199], v[128:131], v[124:127]\n\t" \
    "s_waitcnt lgkmcnt(4)\n\t" \
    "v_mfma_f32_16x16x32_bf16 v[232:235], v[200:203], v[132:135], 0\n\t" \
    "v_mfma_f32_16x16x32_bf16 v[236:239], v[204:207], v[132:135], v[124:127]\n\t" \
    "s_nop 3\n\t" \
    "s_waitcnt lgkmcnt(2)\n\t" \
    "v_mfma_f32_16x16x32_bf16 v[104:107], v[208:211], v[136:139], 0\n\t" \
    "v_mfma_f32_16x16x32_bf16 v[108:111], v[212:215], v[136:139], v[124:127]\n\t" \
    "s_waitcnt lgkmcnt(0)\n\t" \
    "v_mfma_f32_16x16x32_bf16 v[112:115], v[216:219], v[140:143], 0\n\t" \
    "v_mfma_f32_16x16x32_bf16 v[116:119], v[220:223], v[140:143], v[124:127]\n\t" \
    "v_rcp_f32 v120, v231\n\t" \
    "v_rcp_f32 v122, v239\n\t" \
    "ds_read_b128 v[192:195], v13 offset:4864\n\t" \
    "v_pk_mul_f32 v[224:225], v[224:225], v[120:121] op_sel_hi:[1,0]\n\t" \
    "v_pk_mul_f32 v[226:227], v[226:227], v[120:121] op_sel_hi:[1,0]\n\t" \
    "ds_read_b128 v[196:199], v14 offset:4864\n\t" \
    "v_pk_mul_f32 v[228:229], v[228:229], v[120:121] op_sel_hi:[1,0]\n\t" \
    "v_cvt_pk_bf16_f32 v96, v224, v225\n\t" \
    "ds_read_b128 v[200:203], v13 offset:6080\n\t" \
    "v_cvt_pk_bf16_f32 v97, v226, v227\n\t" \
    "v_cvt_pk_bf16_f32 v228, v228, v229\n\t" \
    "ds_read_b128 v[204:207], v15 offset:6080\n\t" \
    "v_or_b32 v52, v52, v228\n\t" \
    "v_pk_mul_f32 v[232:233], v[232:233], v[122:123] op_sel_hi:[1,0]\n\t" \
    "ds_read_b128 v[56:59], v5 offset:0\n\t" \
    "v_pk_mul_f32 v[234:235], v[234:235], v[122:123] op_sel_hi:[1,0]\n\t" \
    "v_pk_mul_f32 v[236:237], v[236:237], v[122:123] op_sel_hi:[1,0]\n\t" \
    "ds_read_b128 v[60:63], v5 offset:1024\n\t" \
    "v_cvt_pk_bf16_f32 v98, v232, v233\n\t" \
    "v_cvt_pk_bf16_f32 v99, v234, v235\n\t" \
    "ds_read_b128 v[64:67], v5 offset:2048\n\t" \
    "v_cvt_pk_bf16_f32 v236, v236, v237\n\t" \
    "v_or_b32 v52, v52, v236\n\t" \
    "ds_read_b128 v[68:71], v5 offset:3072\n\t" \
    "ds_read_b128 v[72:75], v6 offset:4096\n\t" \
    "s_waitcnt lgkmcnt(7)\n\t" \
    "v_mfma_f32_16x16x32_bf16 v[224:227], v[192:195], v[144:147], 0\n\t" \
    "v_rcp_f32 v120, v111\n\t" \
    "v_rcp_f32 v122, v119\n\t" \
    "ds_read_b128 v[208:211], v13 offset:7296\n\t" \
    "v_mfma_f32_16x16x32_bf16 v[228:231], v[196:199], v[144:147], v[124:127]\n\t" \
    "v_pk_mul_f32 v[104:105], v[104:105], v[120:121] op_sel_hi:[1,0]\n\t" \
    "v_pk_mul_f32 v[106:107], v[106:107], v[120:121] op_sel_hi:[1,0]\n\t" \
    "ds_read_b128 v[212:215], v16 offset:7296\n\t" \
    "s_waitcnt lgkmcnt(7)\n\t" \
    "v_mfma_f32_16x16x32_bf16 v[232:235], v[200:203], v[148:151], 0\n\t" \
    "v_pk_mul_f32 v[108:109], v[108:109], v[120:121] op_sel_hi:[1,0]\n\t" \
    "v_cvt_pk_bf16_f32 v100, v104, v105\n\t" \
    "ds_read_b128 v[216:219], v13 offset:8512\n\t" \
    "v_mfma_f32_16x16x32_bf16 v[236:239], v[204:207], v[148:151], v[124:127]\n\t" \
    "v_cvt_pk_bf16_f32 v101, v106, v107\n\t" \
    "v_cvt_pk_bf16_f32 v108, v108, v109\n\t" \
    "ds_read_b128 v[220:223], v17 offset:8512\n\t" \
    "s_waitcnt lgkmcnt(4)\n\t" \
    "v_mfma_f32_16x16x32_bf16 v[20:23], v[56:59], v[96:99], 0\n\t" \
    "v_or_b32 v52, v52, v108\n\t" \
    "v_pk_mul_f32 v[112:113], v[112:113], v[122:123] op_sel_hi:[1,0]\n\t" \
    "ds_read_b128 v[76:79], v5 offset:9216\n\t" \
    "v_mfma_f32_16x16x32_bf16 v[24:27], v[60:63], v[96:99], 0\n\t" \
    "v_pk_mul_f32 v[114:115], v[114:115], v[122:123] op_sel_hi:[1,0]\n\t" \
    "v_pk_mul_f32 v[116:117], v[116:117], v[122:123] op_sel_hi:[1,0]\n\t" \
    "ds_read_b128 v[80:83], v5 offset:10240\n\t" \
    "v_mfma_f32_16x16x32_bf16 v[28:31], v[64:67], v[96:99], 0\n\t" \
    "v_cvt_pk_bf16_f32 v102, v112, v113\n\t" \
    "v_cvt_pk_bf16_f32 v103, v114, v115\n\t" \
    "ds_read_b128 v[84:87], v5 offset:11264\n\t" \
    "v_mfma_f32_16x16x32_bf16 v[32:35], v[68:71], v[96:99], 0\n\t" \
    "v_cvt_pk_bf16_f32 v116, v116, v117\n\t" \
    "v_or_b32 v52, v52, v116\n\t" \
    "ds_read_b128 v[88:91], v5 offset:12288\n\t" \
    "v_mfma_f32_16x16x32_bf16 v[36:39], v[72:75], v[96:99], 0\n\t" \
    "ds_read_b128 v[92:95], v6 offset:13312\n\t" \
    "s_waitcnt lgkmcnt(7)\n\t" \
    "v_mfma_f32_16x16x32_bf16 v[104:107], v[208:211], v[152:155], 0\n\t" \
    "v_rcp_f32 v120, v231\n\t" \
    "v_rcp_f32 v122, v239\n\t" \
    "ds_read_b128 v[192:195], v13 offset:9728\n\t" \
    "v_mfma_f32_16x16x32_bf16 v[108:111], v[212:215], v[152:155], v[124:127]\n\t" \
    "v_pk_mul_f32 v[224:225], v[224:225], v[120:121] op_sel_hi:[1,0]\n\t" \
    "v_pk_mul_f32 v[226:227], v[226:227], v[120:121] op_sel_hi:[1,0]\n\t" \
    "ds_read_b128 v[196:199], v14 offset:9728\n\t" \
    "s_waitcnt lgkmcnt(7)\n\t" \
    "v_mfma_f32_16x16x32_bf16 v[112:115], v[216:219], v[156:159], 0\n\t" \
    "v_pk_mul_f32 v[228:229], v[228:229], v[120:121] op_sel_hi:[1,0]\n\t" \
    "v_cvt_pk_bf16_f32 v96, v224, v225\n\t" \
    "ds_read_b128 v[200:203], v13 offset:10944\n\t" \
    "v_mfma_f32_16x16x32_bf16 v[116:119], v[220:223], v[156:159], v[124:127]\n\t" \
    "v_cvt_pk_bf16_f32 v97, v226, v227\n\t" \
    "v_cvt_pk_bf16_f32 v228, v228, v229\n\t" \
    "ds_read_b128 v[204:207], v15 offset:10944\n\t" \
    "s_waitcnt lgkmcnt(4)\n\t" \
    "v_mfma_f32_16x16x32_bf16 v[20:23], v[76:79], v[100:103], v[20:23]\n\t" \
    "v_or_b32 v53, v53, v228\n\t" \
    "v_pk_mul_f32 v[232:233], v[232:233], v[122:123] op_sel_hi:[1,0]\n\t" \
    "ds_read_b128 v[56:59], v5 offset:18432\n\t" \
    "v_mfma_f32_16x16x32_bf16 v[24:27], v[80:83], v[100:103], v[24:27]\n\t" \
    "v_pk_mul_f32 v[234:235], v[234:235], v[122:123] op_sel_hi:[1,0]\n\t" \
    "v_pk_mul_f32 v[236:237], v[236:237], v[122:123] op_sel_hi:[1,0]\n\t" \
    "ds_read_b128 v[60:63], v5 offset:19456\n\t" \
    "v_mfma_f32_16x16x32_bf16 v[28:31], v[84:87], v[100:103], v[28:31]\n\t" \
    "v_cvt_pk_bf16_f32 v98, v232, v233\n\t" \
    "v_cvt_pk_bf16_f32 v99, v234, v235\n\t" \
    "ds_read_b128 v[64:67], v5 offset:20480\n\t" \
    "v_mfma_f32_16x16x32_bf16 v[32:35], v[88:91], v[100:103], v[32:35]\n\t" \
    "v_cvt_pk_bf16_f32 v236, v236, v237\n\t" \
    "v_or_b32 v53, v53, v236\n\t" \
    "ds_read_b128 v[68:71], v5 offset:21504\n\t" \
    "v_mfma_f32_16x16x32_bf16 v[36:39], v[92:95], v[100:103], v[36:39]\n\t" \
    "ds_read_b128 v[72:75], v6 offset:22528\n\t" \
    "s_waitcnt lgkmcnt(7)\n\t" \
    "v_mfma_f32_16x16x32_bf16 v[224:227], v[192:195], v[160:163], 0\n\t" \
    "v_rcp_f32 v120, v111\n\t" \
    "v_rcp_f32 v122, v119\n\t" \
    "ds_read_b128 v[208:211], v13 offset:12160\n\t" \
    "v_mfma_f32_16x16x32_bf16 v[228:231], v[196:199], v[160:163], v[124:127]\n\t" \
    "v_pk_mul_f32 v[104:105], v[104:105], v[120:121] op_sel_hi:[1,0]\n\t" \
    "v_pk_mul_f32 v[106:107], v[106:107], v[120:121] op_sel_hi:[1,0]\n\t" \
    "ds_read_b128 v[212:215], v16 offset:12160\n\t" \
    "s_waitcnt lgkmcnt(7)\n\t" \
    "v_mfma_f32_16x16x32_bf16 v[232:235], v[200:203], v[164:167], 0\n\t" \
    "v_pk_mul_f32 v[108:109], v[108:109], v[120:121] op_sel_hi:[1,0]\n\t" \
    "v_cvt_pk_bf16_f32 v100, v104, v105\n\t" \
    "ds_read_b128 v[216:219], v13 offset:13376\n\t" \
    "v_mfma_f32_16x16x32_bf16 v[236:239], v[204:207], v[164:167], v[124:127]\n\t" \
    "v_cvt_pk_bf16_f32 v101, v106, v107\n\t" \
    "v_cvt_pk_bf16_f32 v108, v108, v109\n\t" \
    "ds_read_b128 v[220:223], v17 offset:13376\n\t" \
    "s_waitcnt lgkmcnt(4)\n\t" \
    "v_mfma_f32_16x16x32_bf16 v[20:23], v[56:59], v[96:99], v[20:23]\n\t" \
    "v_or_b32 v53, v53, v108\n\t" \
    "v_pk_mul_f32 v[112:113], v[112:113], v[122:123] op_sel_hi:[1,0]\n\t" \
    "ds_read_b128 v[76:79], v5 offset:27648\n\t" \
    "v_mfma_f32_16x16x32_bf16 v[24:27], v[60:63], v[96:99], v[24:27]\n\t" \
    "v_pk_mul_f32 v[114:115], v[114:115], v[122:123] op_sel_hi:[1,0]\n\t" \
    "v_pk_mul_f32 v[116:117], v[116:117], v[122:123] op_sel_hi:[1,0]\n\t" \
    "ds_read_b128 v[80:83], v5 offset:28672\n\t" \
    "v_mfma_f32_16x16x32_bf16 v[28:31], v[64:67], v[96:99], v[28:31]\n\t" \
    "v_cvt_pk_bf16_f32 v102, v112, v113\n\t" \
    "v_cvt_pk_bf16_f32 v103, v114, v115\n\t" \
    "ds_read_b128 v[84:87], v5 offset:29696\n\t" \
    "v_mfma_f32_16x16x32_bf16 v[32:35], v[68:71], v[96:99], v[32:35]\n\t" \
    "v_cvt_pk_bf16_f32 v116, v116, v117\n\t" \
    "v_or_b32 v53, v53, v116\n\t" \
    "ds_read_b128 v[88:91], v5 offset:30720\n\t" \
    "v_mfma_f32_16x16x32_bf16 v[36:39], v[72:75], v[96:99], v[36:39]\n\t" \
    "ds_read_b128 v[92:95], v6 offset:31744\n\t" \
    "ds_read_b64 v[240:241], v7 offset:36864\n\t" \
    "ds_read_b64 v[242:243], v7 offset:37376\n\t" \
    "ds_read_b64 v[244:245], v7 offset:37888\n\t" \
    "ds_read_b64 v[246:247], v7 offset:38400\n\t" \
    "ds_read_b64 v[248:249], v8 offset:38912\n\t" \
    "s_waitcnt lgkmcnt(12)\n\t" \
    "v_mfma_f32_16x16x32_bf16 v[104:107], v[208:211], v[168:171], 0\n\t" \
    "v_rcp_f32 v120, v231\n\t" \
    "v_rcp_f32 v122, v239\n\t" \
    "ds_read_b128 v[192:195], v13 offset:14592\n\t" \
    "v_mfma_f32_16x16x32_bf16 v[108:111], v[212:215], v[168:171], v[124:127]\n\t" \
    "v_pk_mul_f32 v[224:225], v[224:225], v[120:121] op_sel_hi:[1,0]\n\t" \
    "v_pk_mul_f32 v[226:227], v[226:227], v[120:121] op_sel_hi:[1,0]\n\t" \
    "ds_read_b128 v[196:199], v14 offset:14592\n\t" \
    "s_waitcnt lgkmcnt(12)\n\t" \
    "v_mfma_f32_16x16x32_bf16 v[112:115], v[216:219], v[172:175], 0\n\t" \
    "v_pk_mul_f32 v[228:229], v[228:229], v[120:121] op_sel_hi:[1,0]\n\t" \
    "v_cvt_pk_bf16_f32 v96, v224, v225\n\t" \
    "ds_read_b128 v[200:203], v13 offset:15808\n\t" \
    "v_mfma_f32_16x16x32_bf16 v[116:119], v[220:223], v[172:175], v[124:127]\n\t" \
    "v_cvt_pk_bf16_f32 v97, v226, v227\n\t" \
    "v_cvt_pk_bf16_f32 v228, v228, v229\n\t" \
    "ds_read_b128 v[204:207], v15 offset:15808\n\t" \
    "s_waitcnt lgkmcnt(9)\n\t" \
    "v_mfma_f32_16x16x32_bf16 v[20:23], v[76:79], v[100:103], v[20:23]\n\t" \
    "v_or_b32 v54, v54, v228\n\t" \
    "v_pk_mul_f32 v[232:233], v[232:233], v[122:123] op_sel_hi:[1,0]\n\t" \
    "ds_read_b128 v[56:59], v5 offset:41472\n\t" \
    "v_mfma_f32_16x16x32_bf16 v[24:27], v[80:83], v[100:103], v[24:27]\n\t" \
    "v_pk_mul_f32 v[234:235], v[234:235], v[122:123] op_sel_hi:[1,0]\n\t" \
    "v_pk_mul_f32 v[236:237], v[236:237], v[122:123] op_sel_hi:[1,0]\n\t" \
    "ds_read_b128 v[60:63], v5 offset:42496\n\t" \
    "v_mfma_f32_16x16x32_bf16 v[28:31], v[84:87], v[100:103], v[28:31]\n\t" \
    "v_cvt_pk_bf16_f32 v98, v232, v233\n\t" \
    "v_cvt_pk_bf16_f32 v99, v234, v235\n\t" \
    "ds_read_b128 v[64:67], v5 offset:43520\n\t" \
    "v_mfma_f32_16x16x32_bf16 v[32:35], v[88:91], v[100:103], v[32:35]\n\t" \
    "v_cvt_pk_bf16_f32 v236, v236, v237\n\t" \
    "v_or_b32 v54, v54, v236\n\t" \
    "ds_read_b128 v[68:71], v5 offset:44544\n\t" \
    "v_mfma_f32_16x16x32_bf16 v[36:39], v[92:95], v[100:103], v[36:39]\n\t" \
    "ds_read_b128 v[72:75], v6 offset:45568\n\t" \
    "s_waitcnt lgkmcnt(9)\n\t" \
    "v_mfma_f32_16x16x16_bf16 v[20:23], v[240:241], v[52:53], v[20:23]\n\t" \
    "v_mfma_f32_16x16x16_bf16 v[24:27], v[242:243], v[52:53], v[24:27]\n\t" \
    "v_mfma_f32_16x16x16_bf16 v[28:31], v[244:245], v[52:53], v[28:31]\n\t" \
    "v_mfma_f32_16x16x16_bf16 v[32:35], v[246:247], v[52:53], v[32:35]\n\t" \
    "v_mfma_f32_16x16x16_bf16 v[36:39], v[248:249], v[52:53], v[36:39]\n\t" \
    "s_waitcnt lgkmcnt(7)\n\t" \
    "v_mfma_f32_16x16x32_bf16 v[224:227], v[192:195], v[176:179], 0\n\t" \
    "v_rcp_f32 v120, v111\n\t" \
    "v_rcp_f32 v122, v119\n\t" \
    "ds_read_b128 v[208:211], v13 offset:17024\n\t" \
    "v_mfma_f32_16x16x32_bf16 v[228:231], v[196:199], v[176:179], v[124:127]\n\t" \
    "v_pk_mul_f32 v[104:105], v[104:105], v[120:121] op_sel_hi:[1,0]\n\t" \
    "v_pk_mul_f32 v[106:107], v[106:107], v[120:121] op_sel_hi:[1,0]\n\t" \
    "ds_read_b128 v[212:215], v16 offset:17024\n\t" \
    "s_waitcnt lgkmcnt(7)\n\t" \
    "v_mfma_f32_16x16x32_bf16 v[232:235], v[200:203], v[180:183], 0\n\t" \
    "v_pk_mul_f32 v[108:109], v[108:109], v[120:121] op_sel_hi:[1,0]\n\t" \
    "v_cvt_pk_bf16_f32 v100, v104, v105\n\t" \
    "ds_read_b128 v[216:219], v13 offset:18240\n\t" \
    "v_mfma_f32_16x16x32_bf16 v[236:239], v[204:207], v[180:183], v[124:127]\n\t" \
    "v_cvt_pk_bf16_f32 v101, v106, v107\n\t" \
    "v_cvt_pk_bf16_f32 v108, v108, v109\n\t" \
    "ds_read_b128 v[220:223], v17 offset:18240\n\t" \
    "s_waitcnt lgkmcnt(4)\n\t" \
    "v_mfma_f32_16x16x32_bf16 v[20:23], v[56:59], v[96:99], v[20:23]\n\t" \
    "v_or_b32 v54, v54, v108\n\t" \
    "v_pk_mul_f32 v[112:113], v[112:113], v[122:123] op_sel_hi:[1,0]\n\t" \
    "ds_read_b128 v[76:79], v5 offset:50688\n\t" \
    "v_mfma_f32_16x16x32_bf16 v[24:27], v[60:63], v[96:99], v[24:27]\n\t" \
    "v_pk_mul_f32 v[114:115], v[114:115], v[122:123] op_sel_hi:[1,0]\n\t" \
    "v_pk_mul_f32 v[116:117], v[116:117], v[122:123] op_sel_hi:[1,0]\n\t" \
    "ds_read_b128 v[80:83], v5 offset:51712\n\t" \
    "v_mfma_f32_16x16x32_bf16 v[28:31], v[64:67], v[96:99], v[28:31]\n\t" \
    "v_cvt_pk_bf16_f32 v102, v112, v113\n\t" \
    "v_cvt_pk_bf16_f32 v103, v114, v115\n\t" \
    "ds_read_b128 v[84:87], v5 offset:52736\n\t" \
    "v_mfma_f32_16x16x32_bf16 v[32:35], v[68:71], v[96:99], v[32:35]\n\t" \
    "v_cvt_pk_bf16_f32 v116, v116, v117\n\t" \
    "v_or_b32 v54, v54, v116\n\t" \
    "ds_read_b128 v[88:91], v5 offset:53760\n\t" \
    "v_mfma_f32_16x16x32_bf16 v[36:39], v[72:75], v[96:99], v[36:39]\n\t" \
    "ds_read_b128 v[92:95], v6 offset:54784\n\t" \
    "s_waitcnt lgkmcnt(7)\n\t" \
    "v_mfma_f32_16x16x32_bf16 v[104:107], v[208:211], v[184:187], 0\n\t" \
    "v_rcp_f32 v120, v231\n\t" \
    "v_rcp_f32 v122, v239\n\t" \
    "ds_read_b128 v[56:59], v5 offset:59904\n\t" \
    "v_mfma_f32_16x16x32_bf16 v[108:111], v[212:215], v[184:187], v[124:127]\n\t" \
    "v_pk_mul_f32 v[224:225], v[224:225], v[120:121] op_sel_hi:[1,0]\n\t" \
    "v_pk_mul_f32 v[226:227], v[226:227], v[120:121] op_sel_hi:[1,0]\n\t" \
    "ds_read_b128 v[60:63], v5 offset:60928\n\t" \
    "s_waitcnt lgkmcnt(7)\n\t" \
    "v_mfma_f32_16x16x32_bf16 v[112:115], v[216:219], v[188:191], 0\n\t" \
    "v_pk_mul_f32 v[228:229], v[228:229], v[120:121] op_sel_hi:[1,0]\n\t" \
    "v_cvt_pk_bf16_f32 v96, v224, v225\n\t" \
    "ds_read_b128 v[64:67], v5 offset:61952\n\t" \
    "v_mfma_f32_16x16x32_bf16 v[116:119], v[220:223], v[188:191], v[124:127]\n\t" \
    "v_cvt_pk_bf16_f32 v97, v226, v227\n\t" \
    "v_cvt_pk_bf16_f32 v228, v228, v229\n\t" \
    "ds_read_b128 v[68:71], v5 offset:62976\n\t" \
    "s_waitcnt lgkmcnt(4)\n\t" \
    "v_mfma_f32_16x16x32_bf16 v[20:23], v[76:79], v[100:103], v[20:23]\n\t" \
    "v_or_b32 v55, v55, v228\n\t" \
    "v_pk_mul_f32 v[232:233], v[232:233], v[122:123] op_sel_hi:[1,0]\n\t" \
    "ds_read_b128 v[72:75], v6 offset:64000\n\t" \
    "v_mfma_f32_16x16x32_bf16 v[24:27], v[80:83], v[100:103], v[24:27]\n\t" \
    "v_pk_mul_f32 v[234:235], v[234:235], v[122:123] op_sel_hi:[1,0]\n\t" \
    "v_pk_mul_f32 v[236:237], v[236:237], v[122:123] op_sel_hi:[1,0]\n\t" \
    "v_mfma_f32_16x16x32_bf16 v[28:31], v[84:87], v[100:103], v[28:31]\n\t" \
    "v_cvt_pk_bf16_f32 v98, v232, v233\n\t" \
    "v_cvt_pk_bf16_f32 v99, v234, v235\n\t" \
    "v_mfma_f32_16x16x32_bf16 v[32:35], v[88:91], v[100:103], v[32:35]\n\t" \
    "v_cvt_pk_bf16_f32 v236, v236, v237\n\t" \
    "v_or_b32 v55, v55, v236\n\t" \
    "v_mfma_f32_16x16x32_bf16 v[36:39], v[92:95], v[100:103], v[36:39]\n\t" \
    "s_waitcnt lgkmcnt(0)\n\t" \
    "v_mfma_f32_16x16x32_bf16 v[20:23], v[56:59], v[96:99], v[20:23]\n\t" \
    "v_rcp_f32 v120, v111\n\t" \
    "v_rcp_f32 v122, v119\n\t" \
    "v_add_u32 v5, 32768, v5\n\t" \
    "v_add_u32 v6, 32768, v6\n\t" \
    "v_add_u32 v7, 32768, v7\n\t" \
    "v_add_u32 v8, 32768, v8\n\t" \
    "ds_read_b128 v[76:79], v5 offset:36352\n\t" \
    "v_mfma_f32_16x16x32_bf16 v[24:27], v[60:63], v[96:99], v[24:27]\n\t" \
    "v_pk_mul_f32 v[104:105], v[104:105], v[120:121] op_sel_hi:[1,0]\n\t" \
    "v_pk_mul_f32 v[106:107], v[106:107], v[120:121] op_sel_hi:[1,0]\n\t" \
    "ds_read_b128 v[80:83], v5 offset:37376\n\t" \
    "v_mfma_f32_16x16x32_bf16 v[28:31], v[64:67], v[96:99], v[28:31]\n\t" \
    "v_pk_mul_f32 v[108:109], v[108:109], v[120:121] op_sel_hi:[1,0]\n\t" \
    "v_cvt_pk_bf16_f32 v100, v104, v105\n\t" \
    "ds_read_b128 v[84:87], v5 offset:38400\n\t" \
    "v_mfma_f32_16x16x32_bf16 v[32:35], v[68:71], v[96:99], v[32:35]\n\t" \
    "v_cvt_pk_bf16_f32 v101, v106, v107\n\t" \
    "v_cvt_pk_bf16_f32 v108, v108, v109\n\t" \
    "ds_read_b128 v[88:91], v5 offset:39424\n\t" \
    "v_mfma_f32_16x16x32_bf16 v[36:39], v[72:75], v[96:99], v[36:39]\n\t" \
    "v_or_b32 v55, v55, v108\n\t" \
    "v_pk_mul_f32 v[112:113], v[112:113], v[122:123] op_sel_hi:[1,0]\n\t" \
    "ds_read_b128 v[92:95], v6 offset:40448\n\t" \
    "v_pk_mul_f32 v[114:115], v[114:115], v[122:123] op_sel_hi:[1,0]\n\t" \
    "v_pk_mul_f32 v[116:117], v[116:117], v[122:123] op_sel_hi:[1,0]\n\t" \
    "ds_read_b64 v[240:241], v7 offset:45568\n\t" \
    "v_cvt_pk_bf16_f32 v102, v112, v113\n\t" \
    "v_cvt_pk_bf16_f32 v103, v114, v115\n\t" \
    "ds_read_b64 v[242:243], v7 offset:46080\n\t" \
    "v_cvt_pk_bf16_f32 v116, v116, v117\n\t" \
    "v_or_b32 v55, v55, v116\n\t" \
    "ds_read_b64 v[244:245], v7 offset:46592\n\t" \
    "ds_read_b64 v[246:247], v7 offset:47104\n\t" \
    "ds_read_b64 v[248:249], v8 offset:47616\n\t" \
    "s_nop 1\n\t" \
    "s_waitcnt lgkmcnt(5)\n\t" \
    "v_mfma_f32_16x16x32_bf16 v[20:23], v[76:79], v[100:103], v[20:23]\n\t" \
    "v_mfma_f32_16x16x32_bf16 v[24:27], v[80:83], v[100:103], v[24:27]\n\t" \
    "v_mfma_f32_16x16x32_bf16 v[28:31], v[84:87], v[100:103], v[28:31]\n\t" \
    "v_mfma_f32_16x16x32_bf16 v[32:35], v[88:91], v[100:103], v[32:35]\n\t" \
    "v_mfma_f32_16x16x32_bf16 v[36:39], v[92:95], v[100:103], v[36:39]\n\t" \
    "s_waitcnt lgkmcnt(0)\n\t" \
    "v_mfma_f32_16x16x16_bf16 v[20:23], v[240:241], v[54:55], v[20:23]\n\t" \
    "v_mfma_f32_16x16x16_bf16 v[24:27], v[242:243], v[54:55], v[24:27]\n\t" \
    "v_mfma_f32_16x16x16_bf16 v[28:31], v[244:245], v[54:55], v[28:31]\n\t" \
    "v_mfma_f32_16x16x16_bf16 v[32:35], v[246:247], v[54:55], v[32:35]\n\t" \
    "v_mfma_f32_16x16x16_bf16 v[36:39], v[248:249], v[54:55], v[36:39]\n\t" \
    "s_cmp_eq_u32 %[kh], 0\n\t" \
    "s_cbranch_scc1 5f\n\t" \
    "s_nop 5\n\t" \
    "ds_write_b128 v9, v[20:23] offset:0\n\t" \
    "ds_write_b128 v9, v[24:27] offset:1024\n\t" \
    "ds_write_b128 v9, v[28:31] offset:2048\n\t" \
    "ds_write_b128 v9, v[32:35] offset:3072\n\t" \
    "ds_write_b128 v9, v[36:39] offset:4096\n\t" \
    "s_waitcnt lgkmcnt(0)\n\t" \
    "s_setprio 0\n\t" \
    "5:\n\t" \
    "s_barrier\n\t" \
    "ds_read_b128 v[56:59], v9 offset:0\n\t" \
    "ds_read_b128 v[60:63], v9 offset:1024\n\t" \
    "ds_read_b128 v[64:67], v9 offset:2048\n\t" \
    "ds_read_b128 v[68:71], v9 offset:3072\n\t" \
    "ds_read_b128 v[72:75], v9 offset:4096\n\t" \
    "s_waitcnt lgkmcnt(4)\n\t" \
    "v_pk_add_f32 v[20:21], v[20:21], v[56:57]\n\t" \
    "v_pk_add_f32 v[22:23], v[22:23], v[58:59]\n\t" \
    "s_waitcnt lgkmcnt(3)\n\t" \
    "v_pk_add_f32 v[24:25], v[24:25], v[60:61]\n\t" \
    "v_pk_add_f32 v[26:27], v[26:27], v[62:63]\n\t" \
    "s_waitcnt lgkmcnt(2)\n\t" \
    "v_pk_add_f32 v[28:29], v[28:29], v[64:65]\n\t" \
    "v_pk_add_f32 v[30:31], v[30:31], v[66:67]\n\t" \
    "s_waitcnt lgkmcnt(1)\n\t" \
    "v_pk_add_f32 v[32:33], v[32:33], v[68:69]\n\t" \
    "v_pk_add_f32 v[34:35], v[34:35], v[70:71]\n\t" \
    "s_waitcnt lgkmcnt(0)\n\t" \
    "v_pk_add_f32 v[36:37], v[36:37], v[72:73]\n\t" \
    "v_pk_add_f32 v[38:39], v[38:39], v[74:75]\n\t" \
    "s_waitcnt vmcnt(0)\n\t" \
    "s_nop 1\n\t"
#define OPROJ_ASM_B \
    "s_cmp_eq_u32 %[kh], 0\n\t" \
    "s_cbranch_scc1 4f\n\t" \
    "s_setprio 2\n\t" \
    "4:\n\t" \
    "global_load_dwordx4 v[96:99], v1, %[sa] offset:-4096 sc1\n\t" \
    "global_load_dwordx4 v[100:103], v1, %[sa] offset:-3072 sc1\n\t" \
    "global_load_dwordx4 v[104:107], v1, %[sa] offset:-2048 sc1\n\t" \
    "global_load_dwordx4 v[108:111], v1, %[sa] offset:-1024 sc1\n\t" \
    "global_load_dwordx4 v[52:55], v2, %[sax] sc1\n\t" \
    "s_waitcnt vmcnt(5)\n\t" \
    "s_barrier\n\t" \
    "ds_read_b128 v[56:59], v5 offset:0\n\t" \
    "ds_read_b128 v[60:63], v5 offset:1024\n\t" \
    "ds_read_b128 v[64:67], v5 offset:2048\n\t" \
    "ds_read_b128 v[68:71], v5 offset:3072\n\t" \
    "ds_read_b128 v[72:75], v6 offset:4096\n\t" \
    "global_load_dwordx4 v[112:115], v1, %[sa] offset:0 sc1\n\t" \
    "global_load_dwordx4 v[116:119], v1, %[sa] offset:1024 sc1\n\t" \
    "global_load_dwordx4 v[120:123], v1, %[sa] offset:2048 sc1\n\t" \
    "global_load_dwordx4 v[124:127], v1, %[sa] offset:3072 sc1\n\t" \
    "ds_read_b128 v[76:79], v5 offset:9216\n\t" \
    "ds_read_b128 v[80:83], v5 offset:10240\n\t" \
    "ds_read_b128 v[84:87], v5 offset:11264\n\t" \
    "ds_read_b128 v[88:91], v5 offset:12288\n\t" \
    "ds_read_b128 v[92:95], v6 offset:13312\n\t" \
    "s_waitcnt vmcnt(8)\n\t" \
    "s_waitcnt lgkmcnt(9)\n\t" \
    "v_mfma_f32_16x16x32_bf16 v[20:23], v[56:59], v[96:99], 0\n\t" \
    "s_waitcnt lgkmcnt(8)\n\t" \
    "v_mfma_f32_16x16x32_bf16 v[24:27], v[60:63], v[96:99], 0\n\t" \
    "s_waitcnt lgkmcnt(7)\n\t" \
    "v_mfma_f32_16x16x32_bf16 v[28:31], v[64:67], v[96:99], 0\n\t" \
    "s_waitcnt lgkmcnt(6)\n\t" \
    "v_mfma_f32_16x16x32_bf16 v[32:35], v[68:71], v[96:99], 0\n\t" \
    "s_waitcnt lgkmcnt(5)\n\t" \
    "v_mfma_f32_16x16x32_bf16 v[36:39], v[72:75], v[96:99], 0\n\t" \
    "ds_read_b128 v[56:59], v5 offset:18432\n\t" \
    "ds_read_b128 v[60:63], v5 offset:19456\n\t" \
    "ds_read_b128 v[64:67], v5 offset:20480\n\t" \
    "ds_read_b128 v[68:71], v5 offset:21504\n\t" \
    "ds_read_b128 v[72:75], v6 offset:22528\n\t" \
    "s_waitcnt vmcnt(7)\n\t" \
    "s_waitcnt lgkmcnt(9)\n\t" \
    "v_mfma_f32_16x16x32_bf16 v[20:23], v[76:79], v[100:103], v[20:23]\n\t" \
    "s_waitcnt lgkmcnt(8)\n\t" \
    "v_mfma_f32_16x16x32_bf16 v[24:27], v[80:83], v[100:103], v[24:27]\n\t" \
    "s_waitcnt lgkmcnt(7)\n\t" \
    "v_mfma_f32_16x16x32_bf16 v[28:31], v[84:87], v[100:103], v[28:31]\n\t" \
    "s_waitcnt lgkmcnt(6)\n\t" \
    "v_mfma_f32_16x16x32_bf16 v[32:35], v[88:91], v[100:103], v[32:35]\n\t" \
    "s_waitcnt lgkmcnt(5)\n\t" \
    "v_mfma_f32_16x16x32_bf16 v[36:39], v[92:95], v[100:103], v[36:39]\n\t" \
    "ds_read_b128 v[76:79], v5 offset:27648\n\t" \
    "ds_read_b128 v[80:83], v5 offset:28672\n\t" \
    "ds_read_b128 v[84:87], v5 offset:29696\n\t" \
    "ds_read_b128 v[88:91], v5 offset:30720\n\t" \
    "ds_read_b128 v[92:95], v6 offset:31744\n\t" \
    "s_waitcnt vmcnt(6)\n\t" \
    "s_waitcnt lgkmcnt(9)\n\t" \
    "v_mfma_f32_16x16x32_bf16 v[20:23], v[56:59], v[104:107], v[20:23]\n\t" \
    "s_waitcnt lgkmcnt(8)\n\t" \
    "v_mfma_f32_16x16x32_bf16 v[24:27], v[60:63], v[104:107], v[24:27]\n\t" \
    "s_waitcnt lgkmcnt(7)\n\t" \
    "v_mfma_f32_16x16x32_bf16 v[28:31], v[64:67], v[104:107], v[28:31]\n\t" \
    "s_waitcnt lgkmcnt(6)\n\t" \
    "v_mfma_f32_16x16x32_bf16 v[32:35], v[68:71], v[104:107], v[32:35]\n\t" \
    "s_waitcnt lgkmcnt(5)\n\t" \
    "v_mfma_f32_16x16x32_bf16 v[36:39], v[72:75], v[104:107], v[36:39]\n\t" \
    "ds_read_b64 v[56:57], v7 offset:36864\n\t" \
    "ds_read_b64 v[60:61], v7 offset:37376\n\t" \
    "ds_read_b64 v[64:65], v7 offset:37888\n\t" \
    "ds_read_b64 v[68:69], v7 offset:38400\n\t" \
    "ds_read_b64 v[72:73], v8 offset:38912\n\t" \
    "s_waitcnt vmcnt(5)\n\t" \
    "s_waitcnt lgkmcnt(9)\n\t" \
    "v_mfma_f32_16x16x32_bf16 v[20:23], v[76:79], v[108:111], v[20:23]\n\t" \
    "s_waitcnt lgkmcnt(8)\n\t" \
    "v_mfma_f32_16x16x32_bf16 v[24:27], v[80:83], v[108:111], v[24:27]\n\t" \
    "s_waitcnt lgkmcnt(7)\n\t" \
    "v_mfma_f32_16x16x32_bf16 v[28:31], v[84:87], v[108:111], v[28:31]\n\t" \
    "s_waitcnt lgkmcnt(6)\n\t" \
    "v_mfma_f32_16x16x32_bf16 v[32:35], v[88:91], v[108:111], v[32:35]\n\t" \
    "s_waitcnt lgkmcnt(5)\n\t" \
    "v_mfma_f32_16x16x32_bf16 v[36:39], v[92:95], v[108:111], v[36:39]\n\t" \
    "ds_read_b128 v[76:79], v5 offset:41472\n\t" \
    "ds_read_b128 v[80:83], v5 offset:42496\n\t" \
    "ds_read_b128 v[84:87], v5 offset:43520\n\t" \
    "ds_read_b128 v[88:91], v5 offset:44544\n\t" \
    "ds_read_b128 v[92:95], v6 offset:45568\n\t" \
    "s_waitcnt vmcnt(4)\n\t" \
    "s_waitcnt lgkmcnt(9)\n\t" \
    "v_mfma_f32_16x16x16_bf16 v[20:23], v[56:57], v[52:53], v[20:23]\n\t" \
    "s_waitcnt lgkmcnt(8)\n\t" \
    "v_mfma_f32_16x16x16_bf16 v[24:27], v[60:61], v[52:53], v[24:27]\n\t" \
    "s_waitcnt lgkmcnt(7)\n\t" \
    "v_mfma_f32_16x16x16_bf16 v[28:31], v[64:65], v[52:53], v[28:31]\n\t" \
    "s_waitcnt lgkmcnt(6)\n\t" \
    "v_mfma_f32_16x16x16_bf16 v[32:35], v[68:69], v[52:53], v[32:35]\n\t" \
    "s_waitcnt lgkmcnt(5)\n\t" \
    "v_mfma_f32_16x16x16_bf16 v[36:39], v[72:73], v[52:53], v[36:39]\n\t" \
    "ds_read_b128 v[56:59], v5 offset:50688\n\t" \
    "ds_read_b128 v[60:63], v5 offset:51712\n\t" \
    "ds_read_b128 v[64:67], v5 offset:52736\n\t" \
    "ds_read_b128 v[68:71], v5 offset:53760\n\t" \
    "ds_read_b128 v[72:75], v6 offset:54784\n\t" \
    "s_waitcnt vmcnt(3)\n\t" \
    "s_waitcnt lgkmcnt(9)\n\t" \
    "v_mfma_f32_16x16x32_bf16 v[20:23], v[76:79], v[112:115], v[20:23]\n\t" \
    "s_waitcnt lgkmcnt(8)\n\t" \
    "v_mfma_f32_16x16x32_bf16 v[24:27], v[80:83], v[112:115], v[24:27]\n\t" \
    "s_waitcnt lgkmcnt(7)\n\t" \
    "v_mfma_f32_16x16x32_bf16 v[28:31], v[84:87], v[112:115], v[28:31]\n\t" \
    "s_waitcnt lgkmcnt(6)\n\t" \
    "v_mfma_f32_16x16x32_bf16 v[32:35], v[88:91], v[112:115], v[32:35]\n\t" \
    "s_waitcnt lgkmcnt(5)\n\t" \
    "v_mfma_f32_16x16x32_bf16 v[36:39], v[92:95], v[112:115], v[36:39]\n\t" \
    "ds_read_b128 v[76:79], v5 offset:59904\n\t" \
    "ds_read_b128 v[80:83], v5 offset:60928\n\t" \
    "ds_read_b128 v[84:87], v5 offset:61952\n\t" \
    "ds_read_b128 v[88:91], v5 offset:62976\n\t" \
    "ds_read_b128 v[92:95], v6 offset:64000\n\t" \
    "s_waitcnt vmcnt(2)\n\t" \
    "s_waitcnt lgkmcnt(9)\n\t" \
    "v_mfma_f32_16x16x32_bf16 v[20:23], v[56:59], v[116:119], v[20:23]\n\t" \
    "s_waitcnt lgkmcnt(8)\n\t" \
    "v_mfma_f32_16x16x32_bf16 v[24:27], v[60:63], v[116:119], v[24:27]\n\t" \
    "s_waitcnt lgkmcnt(7)\n\t" \
    "v_mfma_f32_16x16x32_bf16 v[28:31], v[64:67], v[116:119], v[28:31]\n\t" \
    "s_waitcnt lgkmcnt(6)\n\t" \
    "v_mfma_f32_16x16x32_bf16 v[32:35], v[68:71], v[116:119], v[32:35]\n\t" \
    "s_waitcnt lgkmcnt(5)\n\t" \
    "v_mfma_f32_16x16x32_bf16 v[36:39], v[72:75], v[116:119], v[36:39]\n\t" \
    "v_add_u32 v5, 32768, v5\n\t" \
    "v_add_u32 v6, 32768, v6\n\t" \
    "v_add_u32 v7, 32768, v7\n\t" \
    "v_add_u32 v8, 32768, v8\n\t" \
    "ds_read_b128 v[56:59], v5 offset:36352\n\t" \
    "ds_read_b128 v[60:63], v5 offset:37376\n\t" \
    "ds_read_b128 v[64:67], v5 offset:38400\n\t" \
    "ds_read_b128 v[68:71], v5 offset:39424\n\t" \
    "ds_read_b128 v[72:75], v6 offset:40448\n\t" \
    "s_waitcnt vmcnt(1)\n\t" \
    "s_waitcnt lgkmcnt(9)\n\t" \
    "v_mfma_f32_16x16x32_bf16 v[20:23], v[76:79], v[120:123], v[20:23]\n\t" \
    "s_waitcnt lgkmcnt(8)\n\t" \
    "v_mfma_f32_16x16x32_bf16 v[24:27], v[80:83], v[120:123], v[24:27]\n\t" \
    "s_waitcnt lgkmcnt(7)\n\t" \
    "v_mfma_f32_16x16x32_bf16 v[28:31], v[84:87], v[120:123], v[28:31]\n\t" \
    "s_waitcnt lgkmcnt(6)\n\t" \
    "v_mfma_f32_16x16x32_bf16 v[32:35], v[88:91], v[120:123], v[32:35]\n\t" \
    "s_waitcnt lgkmcnt(5)\n\t" \
    "v_mfma_f32_16x16x32_bf16 v[36:39], v[92:95], v[120:123], v[36:39]\n\t" \
    "ds_read_b64 v[76:77], v7 offset:45568\n\t" \
    "ds_read_b64 v[80:81], v7 offset:46080\n\t" \
    "ds_read_b64 v[84:85], v7 offset:46592\n\t" \
    "ds_read_b64 v[88:89], v7 offset:47104\n\t" \
    "ds_read_b64 v[92:93], v8 offset:47616\n\t" \
    "s_waitcnt vmcnt(0)\n\t" \
    "s_waitcnt lgkmcnt(9)\n\t" \
    "v_mfma_f32_16x16x32_bf16 v[20:23], v[56:59], v[124:127], v[20:23]\n\t" \
    "s_waitcnt lgkmcnt(8)\n\t" \
    "v_mfma_f32_16x16x32_bf16 v[24:27], v[60:63], v[124:127], v[24:27]\n\t" \
    "s_waitcnt lgkmcnt(7)\n\t" \
    "v_mfma_f32_16x16x32_bf16 v[28:31], v[64:67], v[124:127], v[28:31]\n\t" \
    "s_waitcnt lgkmcnt(6)\n\t" \
    "v_mfma_f32_16x16x32_bf16 v[32:35], v[68:71], v[124:127], v[32:35]\n\t" \
    "s_waitcnt lgkmcnt(5)\n\t" \
    "v_mfma_f32_16x16x32_bf16 v[36:39], v[72:75], v[124:127], v[36:39]\n\t" \
    "s_waitcnt lgkmcnt(4)\n\t" \
    "v_mfma_f32_16x16x16_bf16 v[20:23], v[76:77], v[54:55], v[20:23]\n\t" \
    "s_waitcnt lgkmcnt(3)\n\t" \
    "v_mfma_f32_16x16x16_bf16 v[24:27], v[80:81], v[54:55], v[24:27]\n\t" \
    "s_waitcnt lgkmcnt(2)\n\t" \
    "v_mfma_f32_16x16x16_bf16 v[28:31], v[84:85], v[54:55], v[28:31]\n\t" \
    "s_waitcnt lgkmcnt(1)\n\t" \
    "v_mfma_f32_16x16x16_bf16 v[32:35], v[88:89], v[54:55], v[32:35]\n\t" \
    "s_waitcnt lgkmcnt(0)\n\t" \
    "v_mfma_f32_16x16x16_bf16 v[36:39], v[92:93], v[54:55], v[36:39]\n\t" \
    "s_cmp_eq_u32 %[kh], 0\n\t" \
    "s_cbranch_scc1 5f\n\t" \
    "s_nop 5\n\t" \
    "ds_write_b128 v9, v[20:23] offset:0\n\t" \
    "ds_write_b128 v9, v[24:27] offset:1024\n\t" \
    "ds_write_b128 v9, v[28:31] offset:2048\n\t" \
    "ds_write_b128 v9, v[32:35] offset:3072\n\t" \
    "ds_write_b128 v9, v[36:39] offset:4096\n\t" \
    "s_waitcnt lgkmcnt(0)\n\t" \
    "s_setprio 0\n\t" \
    "5:\n\t" \
    "s_barrier\n\t" \
    "ds_read_b128 v[56:59], v9 offset:0\n\t" \
    "ds_read_b128 v[60:63], v9 offset:1024\n\t" \
    "ds_read_b128 v[64:67], v9 offset:2048\n\t" \
    "ds_read_b128 v[68:71], v9 offset:3072\n\t" \
    "ds_read_b128 v[72:75], v9 offset:4096\n\t" \
    "s_waitcnt lgkmcnt(4)\n\t" \
    "v_pk_add_f32 v[20:21], v[20:21], v[56:57]\n\t" \
    "v_pk_add_f32 v[22:23], v[22:23], v[58:59]\n\t" \
    "s_waitcnt lgkmcnt(3)\n\t" \
    "v_pk_add_f32 v[24:25], v[24:25], v[60:61]\n\t" \
    "v_pk_add_f32 v[26:27], v[26:27], v[62:63]\n\t" \
    "s_waitcnt lgkmcnt(2)\n\t" \
    "v_pk_add_f32 v[28:29], v[28:29], v[64:65]\n\t" \
    "v_pk_add_f32 v[30:31], v[30:31], v[66:67]\n\t" \
    "s_waitcnt lgkmcnt(1)\n\t" \
    "v_pk_add_f32 v[32:33], v[32:33], v[68:69]\n\t" \
    "v_pk_add_f32 v[34:35], v[34:35], v[70:71]\n\t" \
    "s_waitcnt lgkmcnt(0)\n\t" \
    "v_pk_add_f32 v[36:37], v[36:37], v[72:73]\n\t" \
    "v_pk_add_f32 v[38:39], v[38:39], v[74:75]\n\t" \
    "s_waitcnt vmcnt(0)\n\t" \
    "s_nop 1\n\t"

#define OP_CLOB "memory", "scc", "s42", VCLOB10(6),VCLOB10(7),VCLOB10(8),VCLOB10(9),VCLOB10(10),VCLOB10(11),VCLOB10(12),VCLOB10(13),VCLOB10(14), \
    VCLOB10(15),VCLOB10(16),VCLOB10(17),VCLOB10(18),VCLOB10(19),VCLOB10(20),VCLOB10(21),VCLOB10(22),VCLOB10(23), \
    "v52","v53","v54","v55","v56","v57","v58","v59"
#define OP_CLOB_A "memory", "scc", "s42", VCLOB10(6),VCLOB10(7),VCLOB10(8),VCLOB10(9),VCLOB10(10),VCLOB10(11), \
    "v120","v121","v122","v123","v124","v125","v126","v127","v192","v193","v194","v195","v196","v197","v198","v199", \
    VCLOB10(20),VCLOB10(21),VCLOB10(22),VCLOB10(23),VCLOB10(24), "v52","v53","v54","v55","v56","v57","v58","v59"
#define OP_OUTS "={v[20:23]}"(r[0]), "={v[24:27]}"(r[1]), "={v[28:31]}"(r[2]), "={v[32:35]}"(r[3]), "={v[36:39]}"(r[4]), \
    "+{v[40:43]}"(xo0), "+{v[44:47]}"(xo1), "+{v[48:49]}"(xo2), \
    "+{v1}"(va), "+{v5}"(wr), "+{v6}"(wr4), "+{v7}"(xr), "+{v8}"(xr4), "+{v10}"(kg)
#define OPW_OPS "+{s[40:41]}"(wsrc), "={v[40:43]}"(xo0), "={v[44:47]}"(xo1), "={v[48:49]}"(xo2) : "{v3}"(o0), "{v4}"(o2), [sx] "s"(Xold), "{v0}"(vd), "{v12}"(kz), [lb] "s"(lb + w * 1024), [dl] "s"(dl) : "memory", "scc", "s42", "v20", "v21", "v22", "v23"
#define OP_INS "{v2}"(vax), "{v9}"(vex), "{v11}"(kw), "{v13}"(k0), \
    "{v14}"(t1a[0]), "{v15}"(t1a[1]), "{v16}"(t1a[2]), "{v17}"(t1a[3]), \
    [sa] "s"(AM), [sax] "s"(AX), [skv] "s"(kvsrc), [kh] "s"(kh)

template <bool IS_A>
DI void oproj_phase(char* lds, const char* AM, const char* AX, const float* KVB, const char* WO, const char* Xold, char* Xnew,
                    f32x2* stats, uint16_t* out, bool FINAL, unsigned* cnt, unsigned id) {
    int tid = threadIdx.x, bid = blockIdx.x;
    asm volatile("" : "+v"(tid)); asm volatile("" : "+s"(bid));
    const int lane = tid & 63, w = __builtin_amdgcn_readfirstlane(tid >> 6);
    const int r16 = lane & 15, g = lane >> 4;
    const int kh = w >> 2;
    int sb, nb;
    { const int b = bid, xcd = b & 7, i = b >> 3; sb = xcd * 4 + (i & 3); nb = i >> 2; }

    const int stw = sb * 4 + (w & 3);
    const int c0 = nb * 9 + g, c2 = nb * 9 + 8;
    const unsigned o0 = ((stw * 18 + (c0 >> 2)) * 64 + (c0 & 3) * 16 + r16) * 16;
    const unsigned o2 = ((stw * 18 + (c2 >> 2)) * 64 + (c2 & 3) * 16 + r16) * 16 + (g & 1) * 8;
    f32x4 r[5]; u32x4 xo0, xo1; u32x2 xo2;
    {
        const char* wsrc = WO + (size_t)nb * WO_NB + w * 1024;
        const char* kvsrc = (const char*)(KVB + (size_t)(4 * w) * (19 * 20));
        const unsigned lb = (unsigned)(uintptr_t)lds;
        const unsigned dl = w == 0 ? 8192u : 0u;
        const unsigned vd = lane * 16;
        const int l8 = g * 8 + (r16 & 7);
        unsigned wr = lb + kh * 4608 + lane * 16, wr4 = lb + kh * 4608 + l8 * 16;
        unsigned xr = lb + kh * 2304 + lane * 8, xr4 = lb + kh * 2304 + l8 * 8;
        const unsigned vex = lb + 124928 + (w & 3) * 5120 + lane * 16;
        unsigned va, vax, kg = 0, kw = 0, kz = 0, k0 = 0, t1a[4] = {0, 0, 0, 0};
        if constexpr (IS_A) {
            va = (g < 2) ? (stw * 32 + 16 * kh) * 512 + lane * 16 : (unsigned)(AX - AM) + stw * PX_ST + 16 * kh * 64 + r16 * 4;
            vax = g < 2 ? 512 : 64;
            const unsigned kb = lb + (unsigned)WO_NB;
            const int l59 = lane < 59 ? lane : 59, q5 = (l59 * 52) >> 8;
            kg = l59 * 16;
            kw = kb + w * 4864 + q5 * 64 + (l59 - q5 * 5) * 8;
            kz = kb + w * 4864 + lane * 16;
            const unsigned kr = kb + kh * (16 * 1216);
            k0 = kr + r16 * 64 + g * 16;
            const int i4 = r16 & 3, gp = r16 >> 2;
#pragma unroll
            for (int k = 0; k < 4; ++k)
                t1a[k] = kr + ((i4 == 3) ? 18 * 64 + g * 16 : ((gp == k && i4 < 2) ? (16 + i4) * 64 + g * 16 : 48));
            asm volatile(OPROJ_ASM_WA : OPW_OPS);
            typedef __attribute__((ext_vector_type(32))) unsigned u32x32;
            u32x32 pq0, pq1;
            wait_dep(cnt + (C_AG + (sb >> 1)) * 64, 16, lds, id, w);
            asm volatile(OPROJ_ASM_PA : "={v[128:159]}"(pq0), "={v[160:191]}"(pq1), "+{v1}"(va) : "{v2}"(vax), [sa] "s"(AM) : "memory");
            wait_dep(cnt + C_AG * 64, 16, lds, id + 1, w, r16);
            asm volatile(OPROJ_ASM_A : OP_OUTS : OP_INS, "{v[128:159]}"(pq0), "{v[160:191]}"(pq1) : OP_CLOB_A);
        } else {
            va = (stw * 16 + 8 * kh) * 1024 + lane * 16 + 4096;
            vax = (stw * 2 + kh) * 1024 + lane * 16;
            asm volatile(OPROJ_ASM_WB : OPW_OPS);
            wait_dep(cnt + (C_O + (sb >> 2)) * 64, 32, lds, id, w);
            asm volatile(OPROJ_ASM_B : OP_OUTS : OP_INS : OP_CLOB);
        }
    }
    if (!kh) {
    u32x4 y0, y1; u32x2 y2;
    y0[0] = pk2(bflo(xo0[0]) + r[0][0], bfhi(xo0[0]) + r[0][1]);
    y0[1] = pk2(bflo(xo0[1]) + r[0][2], bfhi(xo0[1]) + r[0][3]);
    y0[2] = pk2(bflo(xo0[2]) + r[1][0], bfhi(xo0[2]) + r[1][1]);
    y0[3] = pk2(bflo(xo0[3]) + r[1][2], bfhi(xo0[3]) + r[1][3]);
    y1[0] = pk2(bflo(xo1[0]) + r[2][0], bfhi(xo1[0]) + r[2][1]);
    y1[1] = pk2(bflo(xo1[1]) + r[2][2], bfhi(xo1[1]) + r[2][3]);
    y1[2] = pk2(bflo(xo1[2]) + r[3][0], bfhi(xo1[2]) + r[3][1]);
    y1[3] = pk2(bflo(xo1[3]) + r[3][2], bfhi(xo1[3]) + r[3][3]);
    y2[0] = pk2(bflo(xo2[0]) + r[4][0], bfhi(xo2[0]) + r[4][1]);
    y2[1] = pk2(bflo(xo2[1]) + r[4][2], bfhi(xo2[1]) + r[4][3]);
    if (FINAL) {
        uint16_t* orow = out + (size_t)(stw * 16 + r16) * D_ + nb * 72;
        *(u32x4*)(orow + g * 8) = y0;
        *(u32x4*)(orow + 32 + g * 8) = y1;
        if (g < 2) *(u32x2*)(orow + 64 + g * 4) = y2;
    } else {
        st16(Xnew + o0, y0); st16(Xnew + o0 + 1024, y1);
        if (g < 2) st8(Xnew + o2, y2);
        const float k2 = g < 2 ? 1.0f : 0.0f;
        float s = 0.f;
#pragma unroll
        for (int j = 0; j < 4; ++j) s += (bflo(y0[j]) + bfhi(y0[j])) + (bflo(y1[j]) + bfhi(y1[j]));
        const float e0 = g < 2 ? bflo(y2[0]) : 0.f, e1 = g < 2 ? bfhi(y2[0]) : 0.f, e2 = g < 2 ? bflo(y2[1]) : 0.f, e3 = g < 2 ? bfhi(y2[1]) : 0.f;
        s += (e0 + e1) + (e2 + e3);
        s = sum_x32(sum_x16(s));
        const float mu = s * (1.0f / 72.0f);
        float m2 = 0.f;
#pragma unroll
        for (int j = 0; j < 4; ++j) {
            float a = bflo(y0[j]) - mu, b = bfhi(y0[j]) - mu, c = bflo(y1[j]) - mu, d = bfhi(y1[j]) - mu;
            m2 += (a * a + b * b) + (c * c + d * d);
        }
        {
            float a = e0 - mu, b = e1 - mu, c = e2 - mu, d = e3 - mu;
            m2 += k2 * ((a * a + b * b) + (c * c + d * d));
        }
        m2 = sum_x32(sum_x16(m2));
        if (g == 0) st8(stats + (stw * 16 + r16) * 8 + nb, (u32x2){__float_as_uint(s), __float_as_uint(m2)});
    }
    }
    drain_and_sync();
    if (tid == 0) cnt_add(cnt + ((IS_A ? C_X1 : C_X2) + (sb >> 1)) * 64);
}

DI float max16(const f32x16& s) {
    float a = fmaxf(fmaxf(s[0], s[1]), s[2]), b = fmaxf(fmaxf(s[3], s[4]), s[5]);
    a = fmaxf(fmaxf(a, s[6]), s[7]); b = fmaxf(fmaxf(b, s[8]), s[9]);
    a = fmaxf(fmaxf(a, s[10]), s[11]); b = fmaxf(fmaxf(b, s[12]), s[13]);
    a = fmaxf(fmaxf(a, s[14]), s[15]);
    return fmaxf(a, b);
}
DI float xhalf_max(float m) {
    auto rr = __builtin_amdgcn_permlane32_swap(__float_as_uint(m), __float_as_uint(m), false, false);
    return fmaxf(__uint_as_float(rr[0]), __uint_as_float(rr[1]));
}
DI f32x16 splat16(float v) { f32x16 r;
#pragma unroll
    for (int i = 0; i < 16; ++i) r[i] = v;
    return r; }

#define ATTN_NPEEL 2
#define SC1S " sc1"
#define ATTN_LOOP_ASM \
    "ds_read2_b32 v[132:133], v137 offset0:0 offset1:32\n\t" \
    "ds_read_b128 v[120:123], v136\n\t" \
    "ds_read_b128 v[124:127], v138 offset:0\n\t" \
    "v_mov_b32 v100, 0\n\t" \
    "v_mov_b32 v101, 0\n\t" \
    "v_mov_b32 v102, 0\n\t" \
    "v_mov_b32 v103, 0\n\t" \
    "v_mov_b32 v128, 0\n\t" \
    "v_mov_b32 v129, 0\n\t" \
    "v_mov_b32 v130, 0\n\t" \
    "v_mov_b32 v131, 0\n\t" \
    "s_waitcnt lgkmcnt(1)\n\t" \
    "v_mfma_f32_32x32x8_bf16 v[64:79], v[132:133], v[112:113], v[32:47]\n\t" \
    "v_mfma_f32_32x32x16_bf16 v[64:79], v[120:123], v[104:107], v[64:79]\n\t" \
    "s_nop 7\n\t" \
    "s_nop 3\n\t" \
    "s_mov_b32 s42, m0\n\t" \
    "s_mov_b32 m0, %[lb]\n\t" \
    "v_exp_f32 v64, v64\n\t" \
    "v_exp_f32 v65, v65\n\t" \
    "v_mfma_f32_32x32x16_bf16 v[16:31], v[128:131], v[100:103], v[16:31]\n\t" \
    "ds_read_b128 v[128:131], v138 offset:640\n\t" \
    "v_exp_f32 v66, v66\n\t" \
    "v_exp_f32 v67, v67\n\t" \
    "v_exp_f32 v68, v68\n\t" \
    "v_exp_f32 v69, v69\n\t" \
    "v_mfma_f32_32x32x8_bf16 v[80:95], v[132:133], v[116:117], v[48:63]\n\t" \
    "global_load_lds_dwordx4 %[voff], s[40:41] sc1\n\t" \
    "s_add_u32 m0, m0, 4096\n\t" \
    "s_add_u32 s40, s40, 4096\n\t" \
    "s_addc_u32 s41, s41, 0\n\t" \
    "v_exp_f32 v70, v70\n\t" \
    "v_exp_f32 v71, v71\n\t" \
    "v_exp_f32 v72, v72\n\t" \
    "v_exp_f32 v73, v73\n\t" \
    "v_mfma_f32_32x32x16_bf16 v[80:95], v[120:123], v[108:111], v[80:95]\n\t" \
    "ds_read_b128 v[120:123], v136 offset:1024\n\t" \
    "v_cvt_pk_bf16_f32 v96, v64, v65\n\t" \
    "v_cvt_pk_bf16_f32 v97, v66, v67\n\t" \
    "v_cvt_pk_bf16_f32 v98, v68, v69\n\t" \
    "v_cvt_pk_bf16_f32 v99, v70, v71\n\t" \
    "v_exp_f32 v74, v74\n\t" \
    "v_exp_f32 v75, v75\n\t" \
    "v_exp_f32 v76, v76\n\t" \
    "s_waitcnt lgkmcnt(2)\n\t" \
    "v_mfma_f32_32x32x16_bf16 v[0:15], v[124:127], v[96:99], v[0:15]\n\t" \
    "v_exp_f32 v77, v77\n\t" \
    "v_exp_f32 v78, v78\n\t" \
    "v_exp_f32 v79, v79\n\t" \
    "v_cvt_pk_bf16_f32 v100, v72, v73\n\t" \
    "v_cvt_pk_bf16_f32 v101, v74, v75\n\t" \
    "v_cvt_pk_bf16_f32 v102, v76, v77\n\t" \
    "v_cvt_pk_bf16_f32 v103, v78, v79\n\t" \
    "s_waitcnt lgkmcnt(1)\n\t" \
    "v_exp_f32 v80, v80\n\t" \
    "v_exp_f32 v81, v81\n\t" \
    "v_mfma_f32_32x32x16_bf16 v[0:15], v[128:131], v[100:103], v[0:15]\n\t" \
    "v_exp_f32 v82, v82\n\t" \
    "v_exp_f32 v83, v83\n\t" \
    "v_exp_f32 v84, v84\n\t" \
    "v_exp_f32 v85, v85\n\t" \
    "s_waitcnt lgkmcnt(0)\n\t" \
    "v_mfma_f32_32x32x8_bf16 v[64:79], v[132:133], v[114:115], v[32:47]\n\t" \
    "global_load_lds_dwordx4 %[voff], s[40:41] sc1\n\t" \
    "s_add_u32 m0, m0, 4096\n\t" \
    "s_add_u32 s40, s40, 4096\n\t" \
    "s_addc_u32 s41, s41, 0\n\t" \
    "v_exp_f32 v86, v86\n\t" \
    "v_exp_f32 v87, v87\n\t" \
    "v_exp_f32 v88, v88\n\t" \
    "v_exp_f32 v89, v89\n\t" \
    "v_mfma_f32_32x32x16_bf16 v[64:79], v[120:123], v[104:107], v[64:79]\n\t" \
    "v_cvt_pk_bf16_f32 v96, v80, v81\n\t" \
    "v_cvt_pk_bf16_f32 v97, v82, v83\n\t" \
    "v_cvt_pk_bf16_f32 v98, v84, v85\n\t" \
    "v_cvt_pk_bf16_f32 v99, v86, v87\n\t" \
    "v_exp_f32 v90, v90\n\t" \
    "v_exp_f32 v91, v91\n\t" \
    "v_exp_f32 v92, v92\n\t" \
    "v_mfma_f32_32x32x16_bf16 v[16:31], v[124:127], v[96:99], v[16:31]\n\t" \
    "ds_read_b128 v[124:127], v138 offset:1280\n\t" \
    "v_exp_f32 v93, v93\n\t" \
    "v_exp_f32 v94, v94\n\t" \
    "v_exp_f32 v95, v95\n\t" \
    "v_cvt_pk_bf16_f32 v100, v88, v89\n\t" \
    "v_cvt_pk_bf16_f32 v101, v90, v91\n\t" \
    "v_cvt_pk_bf16_f32 v102, v92, v93\n\t" \
    "v_cvt_pk_bf16_f32 v103, v94, v95\n\t" \
    "v_exp_f32 v64, v64\n\t" \
    "v_exp_f32 v65, v65\n\t" \
    "v_mfma_f32_32x32x16_bf16 v[16:31], v[128:131], v[100:103], v[16:31]\n\t" \
    "ds_read_b128 v[128:131], v138 offset:1920\n\t" \
    "v_exp_f32 v66, v66\n\t" \
    "v_exp_f32 v67, v67\n\t" \
    "v_exp_f32 v68, v68\n\t" \
    "v_exp_f32 v69, v69\n\t" \
    "v_mfma_f32_32x32x8_bf16 v[80:95], v[132:133], v[118:119], v[48:63]\n\t" \
    "global_load_lds_dwordx4 %[voff], s[40:41] sc1\n\t" \
    "s_add_u32 m0, m0, 4096\n\t" \
    "s_add_u32 s40, s40, 4096\n\t" \
    "s_addc_u32 s41, s41, 0\n\t" \
    "v_exp_f32 v70, v70\n\t" \
    "v_exp_f32 v71, v71\n\t" \
    "v_exp_f32 v72, v72\n\t" \
    "v_exp_f32 v73, v73\n\t" \
    "v_mfma_f32_32x32x16_bf16 v[80:95], v[120:123], v[108:111], v[80:95]\n\t" \
    "ds_read_b128 v[120:123], v136 offset:2048\n\t" \
    "ds_read2_b32 v[132:133], v137 offset0:64 offset1:96\n\t" \
    "v_cvt_pk_bf16_f32 v96, v64, v65\n\t" \
    "v_cvt_pk_bf16_f32 v97, v66, v67\n\t" \
    "v_cvt_pk_bf16_f32 v98, v68, v69\n\t" \
    "v_cvt_pk_bf16_f32 v99, v70, v71\n\t" \
    "v_exp_f32 v74, v74\n\t" \
    "v_exp_f32 v75, v75\n\t" \
    "v_exp_f32 v76, v76\n\t" \
    "s_waitcnt lgkmcnt(3)\n\t" \
    "v_mfma_f32_32x32x16_bf16 v[0:15], v[124:127], v[96:99], v[0:15]\n\t" \
    "v_exp_f32 v77, v77\n\t" \
    "v_exp_f32 v78, v78\n\t" \
    "v_exp_f32 v79, v79\n\t" \
    "v_cvt_pk_bf16_f32 v100, v72, v73\n\t" \
    "v_cvt_pk_bf16_f32 v101, v74, v75\n\t" \
    "v_cvt_pk_bf16_f32 v102, v76, v77\n\t" \
    "v_cvt_pk_bf16_f32 v103, v78, v79\n\t" \
    "s_waitcnt lgkmcnt(2)\n\t" \
    "v_exp_f32 v80, v80\n\t" \
    "v_exp_f32 v81, v81\n\t" \
    "v_mfma_f32_32x32x16_bf16 v[0:15], v[128:131], v[100:103], v[0:15]\n\t" \
    "v_exp_f32 v82, v82\n\t" \
    "v_exp_f32 v83, v83\n\t" \
    "v_exp_f32 v84, v84\n\t" \
    "v_exp_f32 v85, v85\n\t" \
    "s_waitcnt lgkmcnt(0)\n\t" \
    "v_mfma_f32_32x32x8_bf16 v[64:79], v[132:133], v[112:113], v[32:47]\n\t" \
    "global_load_lds_dwordx4 %[voff], s[40:41] sc1\n\t" \
    "s_add_u32 m0, m0, 4096\n\t" \
    "s_add_u32 s40, s40, 4096\n\t" \
    "s_addc_u32 s41, s41, 0\n\t" \
    "v_exp_f32 v86, v86\n\t" \
    "v_exp_f32 v87, v87\n\t" \
    "v_exp_f32 v88, v88\n\t" \
    "v_exp_f32 v89, v89\n\t" \
    "v_mfma_f32_32x32x16_bf16 v[64:79], v[120:123], v[104:107], v[64:79]\n\t" \
    "v_cvt_pk_bf16_f32 v96, v80, v81\n\t" \
    "v_cvt_pk_bf16_f32 v97, v82, v83\n\t" \
    "v_cvt_pk_bf16_f32 v98, v84, v85\n\t" \
    "v_cvt_pk_bf16_f32 v99, v86, v87\n\t" \
    "v_exp_f32 v90, v90\n\t" \
    "v_exp_f32 v91, v91\n\t" \
    "v_exp_f32 v92, v92\n\t" \
    "v_mfma_f32_32x32x16_bf16 v[16:31], v[124:127], v[96:99], v[16:31]\n\t" \
    "ds_read_b128 v[124:127], v138 offset:2560\n\t" \
    "v_exp_f32 v93, v93\n\t" \
    "v_exp_f32 v94, v94\n\t" \
    "v_exp_f32 v95, v95\n\t" \
    "v_cvt_pk_bf16_f32 v100, v88, v89\n\t" \
    "v_cvt_pk_bf16_f32 v101, v90, v91\n\t" \
    "v_cvt_pk_bf16_f32 v102, v92, v93\n\t" \
    "v_cvt_pk_bf16_f32 v103, v94, v95\n\t" \
    "v_exp_f32 v64, v64\n\t" \
    "v_exp_f32 v65, v65\n\t" \
    "v_mfma_f32_32x32x16_bf16 v[16:31], v[128:131], v[100:103], v[16:31]\n\t" \
    "ds_read_b128 v[128:131], v138 offset:3200\n\t" \
    "v_exp_f32 v66, v66\n\t" \
    "v_exp_f32 v67, v67\n\t" \
    "v_exp_f32 v68, v68\n\t" \
    "v_exp_f32 v69, v69\n\t" \
    "v_mfma_f32_32x32x8_bf16 v[80:95], v[132:133], v[116:117], v[48:63]\n\t" \
    "global_load_lds_dwordx4 %[voff], s[40:41] sc1\n\t" \
    "s_add_u32 m0, m0, 4096\n\t" \
    "s_add_u32 s40, s40, 4096\n\t" \
    "s_addc_u32 s41, s41, 0\n\t" \
    "v_exp_f32 v70, v70\n\t" \
    "v_exp_f32 v71, v71\n\t" \
    "v_exp_f32 v72, v72\n\t" \
    "v_exp_f32 v73, v73\n\t" \
    "v_mfma_f32_32x32x16_bf16 v[80:95], v[120:123], v[108:111], v[80:95]\n\t" \
    "ds_read_b128 v[120:123], v136 offset:3072\n\t" \
    "v_cvt_pk_bf16_f32 v96, v64, v65\n\t" \
    "v_cvt_pk_bf16_f32 v97, v66, v67\n\t" \
    "v_cvt_pk_bf16_f32 v98, v68, v69\n\t" \
    "v_cvt_pk_bf16_f32 v99, v70, v71\n\t" \
    "v_exp_f32 v74, v74\n\t" \
    "v_exp_f32 v75, v75\n\t" \
    "v_exp_f32 v76, v76\n\t" \
    "s_waitcnt lgkmcnt(2)\n\t" \
    "v_mfma_f32_32x32x16_bf16 v[0:15], v[124:127], v[96:99], v[0:15]\n\t" \
    "v_exp_f32 v77, v77\n\t" \
    "v_exp_f32 v78, v78\n\t" \
    "v_exp_f32 v79, v79\n\t" \
    "v_cvt_pk_bf16_f32 v100, v72, v73\n\t" \
    "v_cvt_pk_bf16_f32 v101, v74, v75\n\t" \
    "v_cvt_pk_bf16_f32 v102, v76, v77\n\t" \
    "v_cvt_pk_bf16_f32 v103, v78, v79\n\t" \
    "s_waitcnt lgkmcnt(1)\n\t" \
    "v_exp_f32 v80, v80\n\t" \
    "v_exp_f32 v81, v81\n\t" \
    "v_mfma_f32_32x32x16_bf16 v[0:15], v[128:131], v[100:103], v[0:15]\n\t" \
    "v_exp_f32 v82, v82\n\t" \
    "v_exp_f32 v83, v83\n\t" \
    "v_exp_f32 v84, v84\n\t" \
    "v_exp_f32 v85, v85\n\t" \
    "s_waitcnt lgkmcnt(0)\n\t" \
    "v_mfma_f32_32x32x8_bf16 v[64:79], v[132:133], v[114:115], v[32:47]\n\t" \
    "global_load_lds_dwordx4 %[voff], s[40:41] sc1\n\t" \
    "s_add_u32 m0, m0, 4096\n\t" \
    "s_add_u32 s40, s40, 4096\n\t" \
    "s_addc_u32 s41, s41, 0\n\t" \
    "v_exp_f32 v86, v86\n\t" \
    "v_exp_f32 v87, v87\n\t" \
    "v_exp_f32 v88, v88\n\t" \
    "v_exp_f32 v89, v89\n\t" \
    "v_mfma_f32_32x32x16_bf16 v[64:79], v[120:123], v[104:107], v[64:79]\n\t" \
    "v_cvt_pk_bf16_f32 v96, v80, v81\n\t" \
    "v_cvt_pk_bf16_f32 v97, v82, v83\n\t" \
    "v_cvt_pk_bf16_f32 v98, v84, v85\n\t" \
    "v_cvt_pk_bf16_f32 v99, v86, v87\n\t" \
    "v_exp_f32 v90, v90\n\t" \
    "v_exp_f32 v91, v91\n\t" \
    "v_exp_f32 v92, v92\n\t" \
    "v_mfma_f32_32x32x16_bf16 v[16:31], v[124:127], v[96:99], v[16:31]\n\t" \
    "ds_read_b128 v[124:127], v138 offset:3840\n\t" \
    "v_exp_f32 v93, v93\n\t" \
    "v_exp_f32 v94, v94\n\t" \
    "v_exp_f32 v95, v95\n\t" \
    "v_cvt_pk_bf16_f32 v100, v88, v89\n\t" \
    "v_cvt_pk_bf16_f32 v101, v90, v91\n\t" \
    "v_cvt_pk_bf16_f32 v102, v92, v93\n\t" \
    "v_cvt_pk_bf16_f32 v103, v94, v95\n\t" \
    "s_waitcnt vmcnt(4)\n\t" \
    "s_barrier\n\t" \
    "v_exp_f32 v64, v64\n\t" \
    "v_exp_f32 v65, v65\n\t" \
    "v_mfma_f32_32x32x16_bf16 v[16:31], v[128:131], v[100:103], v[16:31]\n\t" \
    "ds_read_b128 v[128:131], v138 offset:4480\n\t" \
    "v_exp_f32 v66, v66\n\t" \
    "v_exp_f32 v67, v67\n\t" \
    "v_exp_f32 v68, v68\n\t" \
    "v_exp_f32 v69, v69\n\t" \
    "v_mfma_f32_32x32x8_bf16 v[80:95], v[132:133], v[118:119], v[48:63]\n\t" \
    "global_load_lds_dwordx4 %[voff], s[40:41] sc1\n\t" \
    "s_add_u32 m0, m0, 4096\n\t" \
    "s_add_u32 s40, s40, 4096\n\t" \
    "s_addc_u32 s41, s41, 0\n\t" \
    "v_exp_f32 v70, v70\n\t" \
    "v_exp_f32 v71, v71\n\t" \
    "v_exp_f32 v72, v72\n\t" \
    "v_exp_f32 v73, v73\n\t" \
    "v_mfma_f32_32x32x16_bf16 v[80:95], v[120:123], v[108:111], v[80:95]\n\t" \
    "ds_read_b128 v[120:123], v136 offset:9728\n\t" \
    "v_add_u32 v137, 9728, v137\n\t" \
    "ds_read2_b32 v[132:133], v137 offset0:0 offset1:32\n\t" \
    "v_cvt_pk_bf16_f32 v96, v64, v65\n\t" \
    "v_cvt_pk_bf16_f32 v97, v66, v67\n\t" \
    "v_cvt_pk_bf16_f32 v98, v68, v69\n\t" \
    "v_cvt_pk_bf16_f32 v99, v70, v71\n\t" \
    "v_exp_f32 v74, v74\n\t" \
    "v_exp_f32 v75, v75\n\t" \
    "v_exp_f32 v76, v76\n\t" \
    "s_waitcnt lgkmcnt(3)\n\t" \
    "v_mfma_f32_32x32x16_bf16 v[0:15], v[124:127], v[96:99], v[0:15]\n\t" \
    "v_exp_f32 v77, v77\n\t" \
    "v_exp_f32 v78, v78\n\t" \
    "v_exp_f32 v79, v79\n\t" \
    "v_cvt_pk_bf16_f32 v100, v72, v73\n\t" \
    "v_cvt_pk_bf16_f32 v101, v74, v75\n\t" \
    "v_cvt_pk_bf16_f32 v102, v76, v77\n\t" \
    "v_cvt_pk_bf16_f32 v103, v78, v79\n\t" \
    "s_waitcnt lgkmcnt(2)\n\t" \
    "v_exp_f32 v80, v80\n\t" \
    "v_exp_f32 v81, v81\n\t" \
    "v_mfma_f32_32x32x16_bf16 v[0:15], v[128:131], v[100:103], v[0:15]\n\t" \
    "v_exp_f32 v82, v82\n\t" \
    "v_exp_f32 v83, v83\n\t" \
    "v_exp_f32 v84, v84\n\t" \
    "v_exp_f32 v85, v85\n\t" \
    "s_waitcnt lgkmcnt(0)\n\t" \
    "v_mfma_f32_32x32x8_bf16 v[64:79], v[132:133], v[112:113], v[32:47]\n\t" \
    "global_load_lds_dwordx4 %[voff], s[40:41] sc1\n\t" \
    "s_add_u32 m0, m0, 4096\n\t" \
    "s_add_u32 s40, s40, 4096\n\t" \
    "s_addc_u32 s41, s41, 0\n\t" \
    "v_exp_f32 v86, v86\n\t" \
    "v_exp_f32 v87, v87\n\t" \
    "v_exp_f32 v88, v88\n\t" \
    "v_exp_f32 v89, v89\n\t" \
    "v_mfma_f32_32x32x16_bf16 v[64:79], v[120:123], v[104:107], v[64:79]\n\t" \
    "v_cvt_pk_bf16_f32 v96, v80, v81\n\t" \
    "v_cvt_pk_bf16_f32 v97, v82, v83\n\t" \
    "v_cvt_pk_bf16_f32 v98, v84, v85\n\t" \
    "v_cvt_pk_bf16_f32 v99, v86, v87\n\t" \
    "v_exp_f32 v90, v90\n\t" \
    "v_exp_f32 v91, v91\n\t" \
    "v_exp_f32 v92, v92\n\t" \
    "v_mfma_f32_32x32x16_bf16 v[16:31], v[124:127], v[96:99], v[16:31]\n\t" \
    "ds_read_b128 v[124:127], v138 offset:9728\n\t" \
    "v_exp_f32 v93, v93\n\t" \
    "v_exp_f32 v94, v94\n\t" \
    "v_exp_f32 v95, v95\n\t" \
    "v_cvt_pk_bf16_f32 v100, v88, v89\n\t" \
    "v_cvt_pk_bf16_f32 v101, v90, v91\n\t" \
    "v_cvt_pk_bf16_f32 v102, v92, v93\n\t" \
    "v_cvt_pk_bf16_f32 v103, v94, v95\n\t" \
    "v_add_u32 v136, 9728, v136\n\t" \
    "v_add_u32 v138, 9728, v138\n\t" \
    "v_exp_f32 v64, v64\n\t" \
    "v_exp_f32 v65, v65\n\t" \
    "v_mfma_f32_32x32x16_bf16 v[16:31], v[128:131], v[100:103], v[16:31]\n\t" \
    "ds_read_b128 v[128:131], v138 offset:640\n\t" \
    "v_exp_f32 v66, v66\n\t" \
    "v_exp_f32 v67, v67\n\t" \
    "v_exp_f32 v68, v68\n\t" \
    "v_exp_f32 v69, v69\n\t" \
    "v_mfma_f32_32x32x8_bf16 v[80:95], v[132:133], v[116:117], v[48:63]\n\t" \
    "global_load_lds_dwordx4 %[voff], s[40:41] sc1\n\t" \
    "s_add_u32 m0, m0, 4096\n\t" \
    "s_add_u32 s40, s40, 4096\n\t" \
    "s_addc_u32 s41, s41, 0\n\t" \
    "v_exp_f32 v70, v70\n\t" \
    "v_exp_f32 v71, v71\n\t" \
    "v_exp_f32 v72, v72\n\t" \
    "v_exp_f32 v73, v73\n\t" \
    "v_mfma_f32_32x32x16_bf16 v[80:95], v[120:123], v[108:111], v[80:95]\n\t" \
    "ds_read_b128 v[120:123], v136 offset:1024\n\t" \
    "v_cvt_pk_bf16_f32 v96, v64, v65\n\t" \
    "v_cvt_pk_bf16_f32 v97, v66, v67\n\t" \
    "v_cvt_pk_bf16_f32 v98, v68, v69\n\t" \
    "v_cvt_pk_bf16_f32 v99, v70, v71\n\t" \
    "v_exp_f32 v74, v74\n\t" \
    "v_exp_f32 v75, v75\n\t" \
    "v_exp_f32 v76, v76\n\t" \
    "s_waitcnt lgkmcnt(2)\n\t" \
    "v_mfma_f32_32x32x16_bf16 v[0:15], v[124:127], v[96:99], v[0:15]\n\t" \
    "v_exp_f32 v77, v77\n\t" \
    "v_exp_f32 v78, v78\n\t" \
    "v_exp_f32 v79, v79\n\t" \
    "v_cvt_pk_bf16_f32 v100, v72, v73\n\t" \
    "v_cvt_pk_bf16_f32 v101, v74, v75\n\t" \
    "v_cvt_pk_bf16_f32 v102, v76, v77\n\t" \
    "v_cvt_pk_bf16_f32 v103, v78, v79\n\t" \
    "s_waitcnt lgkmcnt(1)\n\t" \
    "v_exp_f32 v80, v80\n\t" \
    "v_exp_f32 v81, v81\n\t" \
    "v_mfma_f32_32x32x16_bf16 v[0:15], v[128:131], v[100:103], v[0:15]\n\t" \
    "v_exp_f32 v82, v82\n\t" \
    "v_exp_f32 v83, v83\n\t" \
    "v_exp_f32 v84, v84\n\t" \
    "v_exp_f32 v85, v85\n\t" \
    "s_waitcnt lgkmcnt(0)\n\t" \
    "v_mfma_f32_32x32x8_bf16 v[64:79], v[132:133], v[114:115], v[32:47]\n\t" \
    "global_load_lds_dwordx4 %[voff], s[40:41] sc1\n\t" \
    "s_add_u32 m0, m0, 4096\n\t" \
    "s_add_u32 s40, s40, 4096\n\t" \
    "s_addc_u32 s41, s41, 0\n\t" \
    "v_exp_f32 v86, v86\n\t" \
    "v_exp_f32 v87, v87\n\t" \
    "v_exp_f32 v88, v88\n\t" \
    "v_exp_f32 v89, v89\n\t" \
    "v_mfma_f32_32x32x16_bf16 v[64:79], v[120:123], v[104:107], v[64:79]\n\t" \
    "v_cvt_pk_bf16_f32 v96, v80, v81\n\t" \
    "v_cvt_pk_bf16_f32 v97, v82, v83\n\t" \
    "v_cvt_pk_bf16_f32 v98, v84, v85\n\t" \
    "v_cvt_pk_bf16_f32 v99, v86, v87\n\t" \
    "v_exp_f32 v90, v90\n\t" \
    "v_exp_f32 v91, v91\n\t" \
    "v_exp_f32 v92, v92\n\t" \
    "v_mfma_f32_32x32x16_bf16 v[16:31], v[124:127], v[96:99], v[16:31]\n\t" \
    "ds_read_b128 v[124:127], v138 offset:1280\n\t" \
    "v_exp_f32 v93, v93\n\t" \
    "v_exp_f32 v94, v94\n\t" \
    "v_exp_f32 v95, v95\n\t" \
    "v_cvt_pk_bf16_f32 v100, v88, v89\n\t" \
    "v_cvt_pk_bf16_f32 v101, v90, v91\n\t" \
    "v_cvt_pk_bf16_f32 v102, v92, v93\n\t" \
    "v_cvt_pk_bf16_f32 v103, v94, v95\n\t" \
    "v_exp_f32 v64, v64\n\t" \
    "v_exp_f32 v65, v65\n\t" \
    "v_mfma_f32_32x32x16_bf16 v[16:31], v[128:131], v[100:103], v[16:31]\n\t" \
    "ds_read_b128 v[128:131], v138 offset:1920\n\t" \
    "v_exp_f32 v66, v66\n\t" \
    "v_exp_f32 v67, v67\n\t" \
    "v_exp_f32 v68, v68\n\t" \
    "v_exp_f32 v69, v69\n\t" \
    "v_mfma_f32_32x32x8_bf16 v[80:95], v[132:133], v[118:119], v[48:63]\n\t" \
    "global_load_lds_dwordx4 %[voff], s[40:41] sc1\n\t" \
    "s_add_u32 m0, m0, 4096\n\t" \
    "s_add_u32 s40, s40, 4096\n\t" \
    "s_addc_u32 s41, s41, 0\n\t" \
    "v_exp_f32 v70, v70\n\t" \
    "v_exp_f32 v71, v71\n\t" \
    "v_exp_f32 v72, v72\n\t" \
    "v_exp_f32 v73, v73\n\t" \
    "v_mfma_f32_32x32x16_bf16 v[80:95], v[120:123], v[108:111], v[80:95]\n\t" \
    "ds_read_b128 v[120:123], v136 offset:2048\n\t" \
    "ds_read2_b32 v[132:133], v137 offset0:64 offset1:96\n\t" \
    "v_cvt_pk_bf16_f32 v96, v64, v65\n\t" \
    "v_cvt_pk_bf16_f32 v97, v66, v67\n\t" \
    "v_cvt_pk_bf16_f32 v98, v68, v69\n\t" \
    "v_cvt_pk_bf16_f32 v99, v70, v71\n\t" \
    "v_exp_f32 v74, v74\n\t" \
    "v_exp_f32 v75, v75\n\t" \
    "v_exp_f32 v76, v76\n\t" \
    "s_waitcnt lgkmcnt(3)\n\t" \
    "v_mfma_f32_32x32x16_bf16 v[0:15], v[124:127], v[96:99], v[0:15]\n\t" \
    "v_exp_f32 v77, v77\n\t" \
    "v_exp_f32 v78, v78\n\t" \
    "v_exp_f32 v79, v79\n\t" \
    "v_cvt_pk_bf16_f32 v100, v72, v73\n\t" \
    "v_cvt_pk_bf16_f32 v101, v74, v75\n\t" \
    "v_cvt_pk_bf16_f32 v102, v76, v77\n\t" \
    "v_cvt_pk_bf16_f32 v103, v78, v79\n\t" \
    "s_waitcnt lgkmcnt(2)\n\t" \
    "v_exp_f32 v80, v80\n\t" \
    "v_exp_f32 v81, v81\n\t" \
    "v_mfma_f32_32x32x16_bf16 v[0:15], v[128:131], v[100:103], v[0:15]\n\t" \
    "v_exp_f32 v82, v82\n\t" \
    "v_exp_f32 v83, v83\n\t" \
    "v_exp_f32 v84, v84\n\t" \
    "v_exp_f32 v85, v85\n\t" \
    "s_waitcnt lgkmcnt(0)\n\t" \
    "v_mfma_f32_32x32x8_bf16 v[64:79], v[132:133], v[112:113], v[32:47]\n\t" \
    "global_load_lds_dwordx4 %[voff], s[40:41] sc1\n\t" \
    "s_add_u32 m0, m0, 4096\n\t" \
    "s_add_u32 s40, s40, 4096\n\t" \
    "s_addc_u32 s41, s41, 0\n\t" \
    "v_exp_f32 v86, v86\n\t" \
    "v_exp_f32 v87, v87\n\t" \
    "v_exp_f32 v88, v88\n\t" \
    "v_exp_f32 v89, v89\n\t" \
    "v_mfma_f32_32x32x16_bf16 v[64:79], v[120:123], v[104:107], v[64:79]\n\t" \
    "v_cvt_pk_bf16_f32 v96, v80, v81\n\t" \
    "v_cvt_pk_bf16_f32 v97, v82, v83\n\t" \
    "v_cvt_pk_bf16_f32 v98, v84, v85\n\t" \
    "v_cvt_pk_bf16_f32 v99, v86, v87\n\t" \
    "v_exp_f32 v90, v90\n\t" \
    "v_exp_f32 v91, v91\n\t" \
    "v_exp_f32 v92, v92\n\t" \
    "v_mfma_f32_32x32x16_bf16 v[16:31], v[124:127], v[96:99], v[16:31]\n\t" \
    "ds_read_b128 v[124:127], v138 offset:2560\n\t" \
    "v_exp_f32 v93, v93\n\t" \
    "v_exp_f32 v94, v94\n\t" \
    "v_exp_f32 v95, v95\n\t" \
    "v_cvt_pk_bf16_f32 v100, v88, v89\n\t" \
    "v_cvt_pk_bf16_f32 v101, v90, v91\n\t" \
    "v_cvt_pk_bf16_f32 v102, v92, v93\n\t" \
    "v_cvt_pk_bf16_f32 v103, v94, v95\n\t" \
    "v_exp_f32 v64, v64\n\t" \
    "v_exp_f32 v65, v65\n\t" \
    "v_mfma_f32_32x32x16_bf16 v[16:31], v[128:131], v[100:103], v[16:31]\n\t" \
    "ds_read_b128 v[128:131], v138 offset:3200\n\t" \
    "v_exp_f32 v66, v66\n\t" \
    "v_exp_f32 v67, v67\n\t" \
    "v_exp_f32 v68, v68\n\t" \
    "v_exp_f32 v69, v69\n\t" \
    "v_mfma_f32_32x32x8_bf16 v[80:95], v[132:133], v[116:117], v[48:63]\n\t" \
    "global_load_lds_dwordx4 %[voff], s[40:41] sc1\n\t" \
    "s_add_u32 m0, m0, 4096\n\t" \
    "s_add_u32 s40, s40, 4096\n\t" \
    "s_addc_u32 s41, s41, 0\n\t" \
    "v_exp_f32 v70, v70\n\t" \
    "v_exp_f32 v71, v71\n\t" \
    "v_exp_f32 v72, v72\n\t" \
    "v_exp_f32 v73, v73\n\t" \
    "v_mfma_f32_32x32x16_bf16 v[80:95], v[120:123], v[108:111], v[80:95]\n\t" \
    "ds_read_b128 v[120:123], v136 offset:3072\n\t" \
    "v_cvt_pk_bf16_f32 v96, v64, v65\n\t" \
    "v_cvt_pk_bf16_f32 v97, v66, v67\n\t" \
    "v_cvt_pk_bf16_f32 v98, v68, v69\n\t" \
    "v_cvt_pk_bf16_f32 v99, v70, v71\n\t" \
    "v_exp_f32 v74, v74\n\t" \
    "v_exp_f32 v75, v75\n\t" \
    "v_exp_f32 v76, v76\n\t" \
    "s_waitcnt lgkmcnt(2)\n\t" \
    "v_mfma_f32_32x32x16_bf16 v[0:15], v[124:127], v[96:99], v[0:15]\n\t" \
    "v_exp_f32 v77, v77\n\t" \
    "v_exp_f32 v78, v78\n\t" \
    "v_exp_f32 v79, v79\n\t" \
    "v_cvt_pk_bf16_f32 v100, v72, v73\n\t" \
    "v_cvt_pk_bf16_f32 v101, v74, v75\n\t" \
    "v_cvt_pk_bf16_f32 v102, v76, v77\n\t" \
    "v_cvt_pk_bf16_f32 v103, v78, v79\n\t" \
    "s_waitcnt lgkmcnt(1)\n\t" \
    "v_exp_f32 v80, v80\n\t" \
    "v_exp_f32 v81, v81\n\t" \
    "v_mfma_f32_32x32x16_bf16 v[0:15], v[128:131], v[100:103], v[0:15]\n\t" \
    "v_exp_f32 v82, v82\n\t" \
    "v_exp_f32 v83, v83\n\t" \
    "v_exp_f32 v84, v84\n\t" \
    "v_exp_f32 v85, v85\n\t" \
    "s_waitcnt lgkmcnt(0)\n\t" \
    "v_mfma_f32_32x32x8_bf16 v[64:79], v[132:133], v[114:115], v[32:47]\n\t" \
    "global_load_lds_dwordx4 %[voff], s[40:41] sc1\n\t" \
    "s_add_u32 m0, m0, 4096\n\t" \
    "s_add_u32 s40, s40, 4096\n\t" \
    "s_addc_u32 s41, s41, 0\n\t" \
    "v_exp_f32 v86, v86\n\t" \
    "v_exp_f32 v87, v87\n\t" \
    "v_exp_f32 v88, v88\n\t" \
    "v_exp_f32 v89, v89\n\t" \
    "v_mfma_f32_32x32x16_bf16 v[64:79], v[120:123], v[104:107], v[64:79]\n\t" \
    "v_cvt_pk_bf16_f32 v96, v80, v81\n\t" \
    "v_cvt_pk_bf16_f32 v97, v82, v83\n\t" \
    "v_cvt_pk_bf16_f32 v98, v84, v85\n\t" \
    "v_cvt_pk_bf16_f32 v99, v86, v87\n\t" \
    "v_exp_f32 v90, v90\n\t" \
    "v_exp_f32 v91, v91\n\t" \
    "v_exp_f32 v92, v92\n\t" \
    "v_mfma_f32_32x32x16_bf16 v[16:31], v[124:127], v[96:99], v[16:31]\n\t" \
    "ds_read_b128 v[124:127], v138 offset:3840\n\t" \
    "v_exp_f32 v93, v93\n\t" \
    "v_exp_f32 v94, v94\n\t" \
    "v_exp_f32 v95, v95\n\t" \
    "v_cvt_pk_bf16_f32 v100, v88, v89\n\t" \
    "v_cvt_pk_bf16_f32 v101, v90, v91\n\t" \
    "v_cvt_pk_bf16_f32 v102, v92, v93\n\t" \
    "v_cvt_pk_bf16_f32 v103, v94, v95\n\t" \
    "s_waitcnt vmcnt(4)\n\t" \
    "s_barrier\n\t" \
    "v_exp_f32 v64, v64\n\t" \
    "v_exp_f32 v65, v65\n\t" \
    "v_mfma_f32_32x32x16_bf16 v[16:31], v[128:131], v[100:103], v[16:31]\n\t" \
    "ds_read_b128 v[128:131], v138 offset:4480\n\t" \
    "v_exp_f32 v66, v66\n\t" \
    "v_exp_f32 v67, v67\n\t" \
    "v_exp_f32 v68, v68\n\t" \
    "v_exp_f32 v69, v69\n\t" \
    "v_mfma_f32_32x32x8_bf16 v[80:95], v[132:133], v[118:119], v[48:63]\n\t" \
    "global_load_lds_dwordx4 %[voff], s[40:41] sc1\n\t" \
    "s_add_u32 m0, m0, 4096\n\t" \
    "s_add_u32 s40, s40, 4096\n\t" \
    "s_addc_u32 s41, s41, 0\n\t" \
    "v_exp_f32 v70, v70\n\t" \
    "v_exp_f32 v71, v71\n\t" \
    "v_exp_f32 v72, v72\n\t" \
    "v_exp_f32 v73, v73\n\t" \
    "v_mfma_f32_32x32x16_bf16 v[80:95], v[120:123], v[108:111], v[80:95]\n\t" \
    "ds_read_b128 v[120:123], v136 offset:9728\n\t" \
    "v_add_u32 v137, 9728, v137\n\t" \
    "ds_read2_b32 v[132:133], v137 offset0:0 offset1:32\n\t" \
    "v_cvt_pk_bf16_f32 v96, v64, v65\n\t" \
    "v_cvt_pk_bf16_f32 v97, v66, v67\n\t" \
    "v_cvt_pk_bf16_f32 v98, v68, v69\n\t" \
    "v_cvt_pk_bf16_f32 v99, v70, v71\n\t" \
    "v_exp_f32 v74, v74\n\t" \
    "v_exp_f32 v75, v75\n\t" \
    "v_exp_f32 v76, v76\n\t" \
    "s_waitcnt lgkmcnt(3)\n\t" \
    "v_mfma_f32_32x32x16_bf16 v[0:15], v[124:127], v[96:99], v[0:15]\n\t" \
    "v_exp_f32 v77, v77\n\t" \
    "v_exp_f32 v78, v78\n\t" \
    "v_exp_f32 v79, v79\n\t" \
    "v_cvt_pk_bf16_f32 v100, v72, v73\n\t" \
    "v_cvt_pk_bf16_f32 v101, v74, v75\n\t" \
    "v_cvt_pk_bf16_f32 v102, v76, v77\n\t" \
    "v_cvt_pk_bf16_f32 v103, v78, v79\n\t" \
    "s_waitcnt lgkmcnt(2)\n\t" \
    "v_exp_f32 v80, v80\n\t" \
    "v_exp_f32 v81, v81\n\t" \
    "v_mfma_f32_32x32x16_bf16 v[0:15], v[128:131], v[100:103], v[0:15]\n\t" \
    "v_exp_f32 v82, v82\n\t" \
    "v_exp_f32 v83, v83\n\t" \
    "v_exp_f32 v84, v84\n\t" \
    "v_exp_f32 v85, v85\n\t" \
    "s_waitcnt lgkmcnt(0)\n\t" \
    "v_mfma_f32_32x32x8_bf16 v[64:79], v[132:133], v[112:113], v[32:47]\n\t" \
    "global_load_lds_dwordx4 %[voff], s[40:41] sc1\n\t" \
    "s_add_u32 m0, m0, 4096\n\t" \
    "s_add_u32 s40, s40, 4096\n\t" \
    "s_addc_u32 s41, s41, 0\n\t" \
    "v_exp_f32 v86, v86\n\t" \
    "v_exp_f32 v87, v87\n\t" \
    "v_exp_f32 v88, v88\n\t" \
    "v_exp_f32 v89, v89\n\t" \
    "v_mfma_f32_32x32x16_bf16 v[64:79], v[120:123], v[104:107], v[64:79]\n\t" \
    "v_cvt_pk_bf16_f32 v96, v80, v81\n\t" \
    "v_cvt_pk_bf16_f32 v97, v82, v83\n\t" \
    "v_cvt_pk_bf16_f32 v98, v84, v85\n\t" \
    "v_cvt_pk_bf16_f32 v99, v86, v87\n\t" \
    "v_exp_f32 v90, v90\n\t" \
    "v_exp_f32 v91, v91\n\t" \
    "v_exp_f32 v92, v92\n\t" \
    "v_mfma_f32_32x32x16_bf16 v[16:31], v[124:127], v[96:99], v[16:31]\n\t" \
    "ds_read_b128 v[124:127], v138 offset:9728\n\t" \
    "v_exp_f32 v93, v93\n\t" \
    "v_exp_f32 v94, v94\n\t" \
    "v_exp_f32 v95, v95\n\t" \
    "v_cvt_pk_bf16_f32 v100, v88, v89\n\t" \
    "v_cvt_pk_bf16_f32 v101, v90, v91\n\t" \
    "v_cvt_pk_bf16_f32 v102, v92, v93\n\t" \
    "v_cvt_pk_bf16_f32 v103, v94, v95\n\t" \
    "v_add_u32 v136, 9728, v136\n\t" \
    "v_add_u32 v138, 9728, v138\n\t" \
    "s_mov_b32 m0, s42\n\t" \
    "v_exp_f32 v64, v64\n\t" \
    "v_exp_f32 v65, v65\n\t" \
    "v_mfma_f32_32x32x16_bf16 v[16:31], v[128:131], v[100:103], v[16:31]\n\t" \
    "ds_read_b128 v[128:131], v138 offset:640\n\t" \
    "v_exp_f32 v66, v66\n\t" \
    "v_exp_f32 v67, v67\n\t" \
    "v_exp_f32 v68, v68\n\t" \
    "v_exp_f32 v69, v69\n\t" \
    "v_mfma_f32_32x32x8_bf16 v[80:95], v[132:133], v[116:117], v[48:63]\n\t" \
    "v_exp_f32 v70, v70\n\t" \
    "v_exp_f32 v71, v71\n\t" \
    "v_exp_f32 v72, v72\n\t" \
    "v_exp_f32 v73, v73\n\t" \
    "v_mfma_f32_32x32x16_bf16 v[80:95], v[120:123], v[108:111], v[80:95]\n\t" \
    "ds_read_b128 v[120:123], v136 offset:1024\n\t" \
    "v_cvt_pk_bf16_f32 v96, v64, v65\n\t" \
    "v_cvt_pk_bf16_f32 v97, v66, v67\n\t" \
    "v_cvt_pk_bf16_f32 v98, v68, v69\n\t" \
    "v_cvt_pk_bf16_f32 v99, v70, v71\n\t" \
    "v_exp_f32 v74, v74\n\t" \
    "v_exp_f32 v75, v75\n\t" \
    "v_exp_f32 v76, v76\n\t" \
    "s_waitcnt lgkmcnt(2)\n\t" \
    "v_mfma_f32_32x32x16_bf16 v[0:15], v[124:127], v[96:99], v[0:15]\n\t" \
    "v_exp_f32 v77, v77\n\t" \
    "v_exp_f32 v78, v78\n\t" \
    "v_exp_f32 v79, v79\n\t" \
    "v_cvt_pk_bf16_f32 v100, v72, v73\n\t" \
    "v_cvt_pk_bf16_f32 v101, v74, v75\n\t" \
    "v_cvt_pk_bf16_f32 v102, v76, v77\n\t" \
    "v_cvt_pk_bf16_f32 v103, v78, v79\n\t" \
    "s_waitcnt lgkmcnt(1)\n\t" \
    "v_exp_f32 v80, v80\n\t" \
    "v_exp_f32 v81, v81\n\t" \
    "v_mfma_f32_32x32x16_bf16 v[0:15], v[128:131], v[100:103], v[0:15]\n\t" \
    "v_exp_f32 v82, v82\n\t" \
    "v_exp_f32 v83, v83\n\t" \
    "v_exp_f32 v84, v84\n\t" \
    "v_exp_f32 v85, v85\n\t" \
    "s_waitcnt lgkmcnt(0)\n\t" \
    "v_mfma_f32_32x32x8_bf16 v[64:79], v[132:133], v[114:115], v[32:47]\n\t" \
    "v_exp_f32 v86, v86\n\t" \
    "v_exp_f32 v87, v87\n\t" \
    "v_exp_f32 v88, v88\n\t" \
    "v_exp_f32 v89, v89\n\t" \
    "v_mfma_f32_32x32x16_bf16 v[64:79], v[120:123], v[104:107], v[64:79]\n\t" \
    "v_cvt_pk_bf16_f32 v96, v80, v81\n\t" \
    "v_cvt_pk_bf16_f32 v97, v82, v83\n\t" \
    "v_cvt_pk_bf16_f32 v98, v84, v85\n\t" \
    "v_cvt_pk_bf16_f32 v99, v86, v87\n\t" \
    "v_exp_f32 v90, v90\n\t" \
    "v_exp_f32 v91, v91\n\t" \
    "v_exp_f32 v92, v92\n\t" \
    "v_mfma_f32_32x32x16_bf16 v[16:31], v[124:127], v[96:99], v[16:31]\n\t" \
    "ds_read_b128 v[124:127], v138 offset:1280\n\t" \
    "v_exp_f32 v93, v93\n\t" \
    "v_exp_f32 v94, v94\n\t" \
    "v_exp_f32 v95, v95\n\t" \
    "v_cvt_pk_bf16_f32 v100, v88, v89\n\t" \
    "v_cvt_pk_bf16_f32 v101, v90, v91\n\t" \
    "v_cvt_pk_bf16_f32 v102, v92, v93\n\t" \
    "v_cvt_pk_bf16_f32 v103, v94, v95\n\t" \
    "v_exp_f32 v64, v64\n\t" \
    "v_exp_f32 v65, v65\n\t" \
    "v_mfma_f32_32x32x16_bf16 v[16:31], v[128:131], v[100:103], v[16:31]\n\t" \
    "ds_read_b128 v[128:131], v138 offset:1920\n\t" \
    "v_exp_f32 v66, v66\n\t" \
    "v_exp_f32 v67, v67\n\t" \
    "v_exp_f32 v68, v68\n\t" \
    "v_exp_f32 v69, v69\n\t" \
    "v_mfma_f32_32x32x8_bf16 v[80:95], v[132:133], v[118:119], v[48:63]\n\t" \
    "v_exp_f32 v70, v70\n\t" \
    "v_exp_f32 v71, v71\n\t" \
    "v_exp_f32 v72, v72\n\t" \
    "v_exp_f32 v73, v73\n\t" \
    "v_mfma_f32_32x32x16_bf16 v[80:95], v[120:123], v[108:111], v[80:95]\n\t" \
    "ds_read_b128 v[120:123], v136 offset:2048\n\t" \
    "ds_read2_b32 v[132:133], v137 offset0:64 offset1:96\n\t" \
    "v_cvt_pk_bf16_f32 v96, v64, v65\n\t" \
    "v_cvt_pk_bf16_f32 v97, v66, v67\n\t" \
    "v_cvt_pk_bf16_f32 v98, v68, v69\n\t" \
    "v_cvt_pk_bf16_f32 v99, v70, v71\n\t" \
    "v_exp_f32 v74, v74\n\t" \
    "v_exp_f32 v75, v75\n\t" \
    "v_exp_f32 v76, v76\n\t" \
    "s_waitcnt lgkmcnt(3)\n\t" \
    "v_mfma_f32_32x32x16_bf16 v[0:15], v[124:127], v[96:99], v[0:15]\n\t" \
    "v_exp_f32 v77, v77\n\t" \
    "v_exp_f32 v78, v78\n\t" \
    "v_exp_f32 v79, v79\n\t" \
    "v_cvt_pk_bf16_f32 v100, v72, v73\n\t" \
    "v_cvt_pk_bf16_f32 v101, v74, v75\n\t" \
    "v_cvt_pk_bf16_f32 v102, v76, v77\n\t" \
    "v_cvt_pk_bf16_f32 v103, v78, v79\n\t" \
    "s_waitcnt lgkmcnt(2)\n\t" \
    "v_exp_f32 v80, v80\n\t" \
    "v_exp_f32 v81, v81\n\t" \
    "v_mfma_f32_32x32x16_bf16 v[0:15], v[128:131], v[100:103], v[0:15]\n\t" \
    "v_exp_f32 v82, v82\n\t" \
    "v_exp_f32 v83, v83\n\t" \
    "v_exp_f32 v84, v84\n\t" \
    "v_exp_f32 v85, v85\n\t" \
    "s_waitcnt lgkmcnt(0)\n\t" \
    "v_mfma_f32_32x32x8_bf16 v[64:79], v[132:133], v[112:113], v[32:47]\n\t" \
    "v_exp_f32 v86, v86\n\t" \
    "v_exp_f32 v87, v87\n\t" \
    "v_exp_f32 v88, v88\n\t" \
    "v_exp_f32 v89, v89\n\t" \
    "v_mfma_f32_32x32x16_bf16 v[64:79], v[120:123], v[104:107], v[64:79]\n\t" \
    "v_cvt_pk_bf16_f32 v96, v80, v81\n\t" \
    "v_cvt_pk_bf16_f32 v97, v82, v83\n\t" \
    "v_cvt_pk_bf16_f32 v98, v84, v85\n\t" \
    "v_cvt_pk_bf16_f32 v99, v86, v87\n\t" \
    "v_exp_f32 v90, v90\n\t" \
    "v_exp_f32 v91, v91\n\t" \
    "v_exp_f32 v92, v92\n\t" \
    "v_mfma_f32_32x32x16_bf16 v[16:31], v[124:127], v[96:99], v[16:31]\n\t" \
    "ds_read_b128 v[124:127], v138 offset:2560\n\t" \
    "v_exp_f32 v93, v93\n\t" \
    "v_exp_f32 v94, v94\n\t" \
    "v_exp_f32 v95, v95\n\t" \
    "v_cvt_pk_bf16_f32 v100, v88, v89\n\t" \
    "v_cvt_pk_bf16_f32 v101, v90, v91\n\t" \
    "v_cvt_pk_bf16_f32 v102, v92, v93\n\t" \
    "v_cvt_pk_bf16_f32 v103, v94, v95\n\t" \
    "v_exp_f32 v64, v64\n\t" \
    "v_exp_f32 v65, v65\n\t" \
    "v_mfma_f32_32x32x16_bf16 v[16:31], v[128:131], v[100:103], v[16:31]\n\t" \
    "ds_read_b128 v[128:131], v138 offset:3200\n\t" \
    "v_exp_f32 v66, v66\n\t" \
    "v_exp_f32 v67, v67\n\t" \
    "v_exp_f32 v68, v68\n\t" \
    "v_exp_f32 v69, v69\n\t" \
    "v_mfma_f32_32x32x8_bf16 v[80:95], v[132:133], v[116:117], v[48:63]\n\t" \
    "v_exp_f32 v70, v70\n\t" \
    "v_exp_f32 v71, v71\n\t" \
    "v_exp_f32 v72, v72\n\t" \
    "v_exp_f32 v73, v73\n\t" \
    "v_mfma_f32_32x32x16_bf16 v[80:95], v[120:123], v[108:111], v[80:95]\n\t" \
    "ds_read_b128 v[120:123], v136 offset:3072\n\t" \
    "v_cvt_pk_bf16_f32 v96, v64, v65\n\t" \
    "v_cvt_pk_bf16_f32 v97, v66, v67\n\t" \
    "v_cvt_pk_bf16_f32 v98, v68, v69\n\t" \
    "v_cvt_pk_bf16_f32 v99, v70, v71\n\t" \
    "v_exp_f32 v74, v74\n\t" \
    "v_exp_f32 v75, v75\n\t" \
    "v_exp_f32 v76, v76\n\t" \
    "s_waitcnt lgkmcnt(2)\n\t" \
    "v_mfma_f32_32x32x16_bf16 v[0:15], v[124:127], v[96:99], v[0:15]\n\t" \
    "v_exp_f32 v77, v77\n\t" \
    "v_exp_f32 v78, v78\n\t" \
    "v_exp_f32 v79, v79\n\t" \
    "v_cvt_pk_bf16_f32 v100, v72, v73\n\t" \
    "v_cvt_pk_bf16_f32 v101, v74, v75\n\t" \
    "v_cvt_pk_bf16_f32 v102, v76, v77\n\t" \
    "v_cvt_pk_bf16_f32 v103, v78, v79\n\t" \
    "s_waitcnt lgkmcnt(1)\n\t" \
    "v_exp_f32 v80, v80\n\t" \
    "v_exp_f32 v81, v81\n\t" \
    "v_mfma_f32_32x32x16_bf16 v[0:15], v[128:131], v[100:103], v[0:15]\n\t" \
    "v_exp_f32 v82, v82\n\t" \
    "v_exp_f32 v83, v83\n\t" \
    "v_exp_f32 v84, v84\n\t" \
    "v_exp_f32 v85, v85\n\t" \
    "s_waitcnt lgkmcnt(0)\n\t" \
    "v_mfma_f32_32x32x8_bf16 v[64:79], v[132:133], v[114:115], v[32:47]\n\t" \
    "v_exp_f32 v86, v86\n\t" \
    "v_exp_f32 v87, v87\n\t" \
    "v_exp_f32 v88, v88\n\t" \
    "v_exp_f32 v89, v89\n\t" \
    "v_mfma_f32_32x32x16_bf16 v[64:79], v[120:123], v[104:107], v[64:79]\n\t" \
    "v_cvt_pk_bf16_f32 v96, v80, v81\n\t" \
    "v_cvt_pk_bf16_f32 v97, v82, v83\n\t" \
    "v_cvt_pk_bf16_f32 v98, v84, v85\n\t" \
    "v_cvt_pk_bf16_f32 v99, v86, v87\n\t" \
    "v_exp_f32 v90, v90\n\t" \
    "v_exp_f32 v91, v91\n\t" \
    "v_exp_f32 v92, v92\n\t" \
    "v_mfma_f32_32x32x16_bf16 v[16:31], v[124:127], v[96:99], v[16:31]\n\t" \
    "ds_read_b128 v[124:127], v138 offset:3840\n\t" \
    "v_exp_f32 v93, v93\n\t" \
    "v_exp_f32 v94, v94\n\t" \
    "v_exp_f32 v95, v95\n\t" \
    "v_cvt_pk_bf16_f32 v100, v88, v89\n\t" \
    "v_cvt_pk_bf16_f32 v101, v90, v91\n\t" \
    "v_cvt_pk_bf16_f32 v102, v92, v93\n\t" \
    "v_cvt_pk_bf16_f32 v103, v94, v95\n\t" \
    "s_waitcnt vmcnt(0)\n\t" \
    "s_barrier\n\t" \
    "v_exp_f32 v64, v64\n\t" \
    "v_exp_f32 v65, v65\n\t" \
    "v_mfma_f32_32x32x16_bf16 v[16:31], v[128:131], v[100:103], v[16:31]\n\t" \
    "ds_read_b128 v[128:131], v138 offset:4480\n\t" \
    "v_exp_f32 v66, v66\n\t" \
    "v_exp_f32 v67, v67\n\t" \
    "v_exp_f32 v68, v68\n\t" \
    "v_exp_f32 v69, v69\n\t" \
    "v_mfma_f32_32x32x8_bf16 v[80:95], v[132:133], v[118:119], v[48:63]\n\t" \
    "v_exp_f32 v70, v70\n\t" \
    "v_exp_f32 v71, v71\n\t" \
    "v_exp_f32 v72, v72\n\t" \
    "v_exp_f32 v73, v73\n\t" \
    "v_mfma_f32_32x32x16_bf16 v[80:95], v[120:123], v[108:111], v[80:95]\n\t" \
    "ds_read_b128 v[120:123], v136 offset:9728\n\t" \
    "v_add_u32 v137, 9728, v137\n\t" \
    "ds_read2_b32 v[132:133], v137 offset0:0 offset1:32\n\t" \
    "v_cvt_pk_bf16_f32 v96, v64, v65\n\t" \
    "v_cvt_pk_bf16_f32 v97, v66, v67\n\t" \
    "v_cvt_pk_bf16_f32 v98, v68, v69\n\t" \
    "v_cvt_pk_bf16_f32 v99, v70, v71\n\t" \
    "v_exp_f32 v74, v74\n\t" \
    "v_exp_f32 v75, v75\n\t" \
    "v_exp_f32 v76, v76\n\t" \
    "s_waitcnt lgkmcnt(3)\n\t" \
    "v_mfma_f32_32x32x16_bf16 v[0:15], v[124:127], v[96:99], v[0:15]\n\t" \
    "v_exp_f32 v77, v77\n\t" \
    "v_exp_f32 v78, v78\n\t" \
    "v_exp_f32 v79, v79\n\t" \
    "v_cvt_pk_bf16_f32 v100, v72, v73\n\t" \
    "v_cvt_pk_bf16_f32 v101, v74, v75\n\t" \
    "v_cvt_pk_bf16_f32 v102, v76, v77\n\t" \
    "v_cvt_pk_bf16_f32 v103, v78, v79\n\t" \
    "s_waitcnt lgkmcnt(2)\n\t" \
    "v_exp_f32 v80, v80\n\t" \
    "v_exp_f32 v81, v81\n\t" \
    "v_mfma_f32_32x32x16_bf16 v[0:15], v[128:131], v[100:103], v[0:15]\n\t" \
    "v_exp_f32 v82, v82\n\t" \
    "v_exp_f32 v83, v83\n\t" \
    "v_exp_f32 v84, v84\n\t" \
    "v_exp_f32 v85, v85\n\t" \
    "s_waitcnt lgkmcnt(0)\n\t" \
    "v_mfma_f32_32x32x8_bf16 v[64:79], v[132:133], v[112:113], v[32:47]\n\t" \
    "v_exp_f32 v86, v86\n\t" \
    "v_exp_f32 v87, v87\n\t" \
    "v_exp_f32 v88, v88\n\t" \
    "v_exp_f32 v89, v89\n\t" \
    "v_mfma_f32_32x32x16_bf16 v[64:79], v[120:123], v[104:107], v[64:79]\n\t" \
    "v_cvt_pk_bf16_f32 v96, v80, v81\n\t" \
    "v_cvt_pk_bf16_f32 v97, v82, v83\n\t" \
    "v_cvt_pk_bf16_f32 v98, v84, v85\n\t" \
    "v_cvt_pk_bf16_f32 v99, v86, v87\n\t" \
    "v_exp_f32 v90, v90\n\t" \
    "v_exp_f32 v91, v91\n\t" \
    "v_exp_f32 v92, v92\n\t" \
    "v_mfma_f32_32x32x16_bf16 v[16:31], v[124:127], v[96:99], v[16:31]\n\t" \
    "ds_read_b128 v[124:127], v138 offset:9728\n\t" \
    "v_exp_f32 v93, v93\n\t" \
    "v_exp_f32 v94, v94\n\t" \
    "v_exp_f32 v95, v95\n\t" \
    "v_cvt_pk_bf16_f32 v100, v88, v89\n\t" \
    "v_cvt_pk_bf16_f32 v101, v90, v91\n\t" \
    "v_cvt_pk_bf16_f32 v102, v92, v93\n\t" \
    "v_cvt_pk_bf16_f32 v103, v94, v95\n\t" \
    "v_add_u32 v136, 9728, v136\n\t" \
    "v_add_u32 v138, 9728, v138\n\t" \
    "s_mov_b32 s42, 5\n\t" \
    "1:\n\t" \
    "v_exp_f32 v64, v64\n\t" \
    "v_exp_f32 v65, v65\n\t" \
    "v_mfma_f32_32x32x16_bf16 v[16:31], v[128:131], v[100:103], v[16:31]\n\t" \
    "ds_read_b128 v[128:131], v138 offset:640\n\t" \
    "v_exp_f32 v66, v66\n\t" \
    "v_exp_f32 v67, v67\n\t" \
    "v_exp_f32 v68, v68\n\t" \
    "v_exp_f32 v69, v69\n\t" \
    "v_mfma_f32_32x32x8_bf16 v[80:95], v[132:133], v[116:117], v[48:63]\n\t" \
    "v_exp_f32 v70, v70\n\t" \
    "v_exp_f32 v71, v71\n\t" \
    "v_exp_f32 v72, v72\n\t" \
    "v_exp_f32 v73, v73\n\t" \
    "v_mfma_f32_32x32x16_bf16 v[80:95], v[120:123], v[108:111], v[80:95]\n\t" \
    "ds_read_b128 v[120:123], v136 offset:1024\n\t" \
    "v_cvt_pk_bf16_f32 v96, v64, v65\n\t" \
    "v_cvt_pk_bf16_f32 v97, v66, v67\n\t" \
    "v_cvt_pk_bf16_f32 v98, v68, v69\n\t" \
    "v_cvt_pk_bf16_f32 v99, v70, v71\n\t" \
    "v_exp_f32 v74, v74\n\t" \
    "v_exp_f32 v75, v75\n\t" \
    "v_exp_f32 v76, v76\n\t" \
    "s_waitcnt lgkmcnt(2)\n\t" \
    "v_mfma_f32_32x32x16_bf16 v[0:15], v[124:127], v[96:99], v[0:15]\n\t" \
    "v_exp_f32 v77, v77\n\t" \
    "v_exp_f32 v78, v78\n\t" \
    "v_exp_f32 v79, v79\n\t" \
    "v_cvt_pk_bf16_f32 v100, v72, v73\n\t" \
    "v_cvt_pk_bf16_f32 v101, v74, v75\n\t" \
    "v_cvt_pk_bf16_f32 v102, v76, v77\n\t" \
    "v_cvt_pk_bf16_f32 v103, v78, v79\n\t" \
    "s_waitcnt lgkmcnt(1)\n\t" \
    "v_exp_f32 v80, v80\n\t" \
    "v_exp_f32 v81, v81\n\t" \
    "v_mfma_f32_32x32x16_bf16 v[0:15], v[128:131], v[100:103], v[0:15]\n\t" \
    "v_exp_f32 v82, v82\n\t" \
    "v_exp_f32 v83, v83\n\t" \
    "v_exp_f32 v84, v84\n\t" \
    "v_exp_f32 v85, v85\n\t" \
    "s_waitcnt lgkmcnt(0)\n\t" \
    "v_mfma_f32_32x32x8_bf16 v[64:79], v[132:133], v[114:115], v[32:47]\n\t" \
    "v_exp_f32 v86, v86\n\t" \
    "v_exp_f32 v87, v87\n\t" \
    "v_exp_f32 v88, v88\n\t" \
    "v_exp_f32 v89, v89\n\t" \
    "v_mfma_f32_32x32x16_bf16 v[64:79], v[120:123], v[104:107], v[64:79]\n\t" \
    "v_cvt_pk_bf16_f32 v96, v80, v81\n\t" \
    "v_cvt_pk_bf16_f32 v97, v82, v83\n\t" \
    "v_cvt_pk_bf16_f32 v98, v84, v85\n\t" \
    "v_cvt_pk_bf16_f32 v99, v86, v87\n\t" \
    "v_exp_f32 v90, v90\n\t" \
    "v_exp_f32 v91, v91\n\t" \
    "v_exp_f32 v92, v92\n\t" \
    "v_mfma_f32_32x32x16_bf16 v[16:31], v[124:127], v[96:99], v[16:31]\n\t" \
    "ds_read_b128 v[124:127], v138 offset:1280\n\t" \
    "v_exp_f32 v93, v93\n\t" \
    "v_exp_f32 v94, v94\n\t" \
    "v_exp_f32 v95, v95\n\t" \
    "v_cvt_pk_bf16_f32 v100, v88, v89\n\t" \
    "v_cvt_pk_bf16_f32 v101, v90, v91\n\t" \
    "v_cvt_pk_bf16_f32 v102, v92, v93\n\t" \
    "v_cvt_pk_bf16_f32 v103, v94, v95\n\t" \
    "v_exp_f32 v64, v64\n\t" \
    "v_exp_f32 v65, v65\n\t" \
    "v_mfma_f32_32x32x16_bf16 v[16:31], v[128:131], v[100:103], v[16:31]\n\t" \
    "ds_read_b128 v[128:131], v138 offset:1920\n\t" \
    "v_exp_f32 v66, v66\n\t" \
    "v_exp_f32 v67, v67\n\t" \
    "v_exp_f32 v68, v68\n\t" \
    "v_exp_f32 v69, v69\n\t" \
    "v_mfma_f32_32x32x8_bf16 v[80:95], v[132:133], v[118:119], v[48:63]\n\t" \
    "v_exp_f32 v70, v70\n\t" \
    "v_exp_f32 v71, v71\n\t" \
    "v_exp_f32 v72, v72\n\t" \
    "v_exp_f32 v73, v73\n\t" \
    "v_mfma_f32_32x32x16_bf16 v[80:95], v[120:123], v[108:111], v[80:95]\n\t" \
    "ds_read_b128 v[120:123], v136 offset:2048\n\t" \
    "ds_read2_b32 v[132:133], v137 offset0:64 offset1:96\n\t" \
    "v_cvt_pk_bf16_f32 v96, v64, v65\n\t" \
    "v_cvt_pk_bf16_f32 v97, v66, v67\n\t" \
    "v_cvt_pk_bf16_f32 v98, v68, v69\n\t" \
    "v_cvt_pk_bf16_f32 v99, v70, v71\n\t" \
    "v_exp_f32 v74, v74\n\t" \
    "v_exp_f32 v75, v75\n\t" \
    "v_exp_f32 v76, v76\n\t" \
    "s_waitcnt lgkmcnt(3)\n\t" \
    "v_mfma_f32_32x32x16_bf16 v[0:15], v[124:127], v[96:99], v[0:15]\n\t" \
    "v_exp_f32 v77, v77\n\t" \
    "v_exp_f32 v78, v78\n\t" \
    "v_exp_f32 v79, v79\n\t" \
    "v_cvt_pk_bf16_f32 v100, v72, v73\n\t" \
    "v_cvt_pk_bf16_f32 v101, v74, v75\n\t" \
    "v_cvt_pk_bf16_f32 v102, v76, v77\n\t" \
    "v_cvt_pk_bf16_f32 v103, v78, v79\n\t" \
    "s_waitcnt lgkmcnt(2)\n\t" \
    "v_exp_f32 v80, v80\n\t" \
    "v_exp_f32 v81, v81\n\t" \
    "v_mfma_f32_32x32x16_bf16 v[0:15], v[128:131], v[100:103], v[0:15]\n\t" \
    "v_exp_f32 v82, v82\n\t" \
    "v_exp_f32 v83, v83\n\t" \
    "v_exp_f32 v84, v84\n\t" \
    "v_exp_f32 v85, v85\n\t" \
    "s_waitcnt lgkmcnt(0)\n\t" \
    "v_mfma_f32_32x32x8_bf16 v[64:79], v[132:133], v[112:113], v[32:47]\n\t" \
    "v_exp_f32 v86, v86\n\t" \
    "v_exp_f32 v87, v87\n\t" \
    "v_exp_f32 v88, v88\n\t" \
    "v_exp_f32 v89, v89\n\t" \
    "v_mfma_f32_32x32x16_bf16 v[64:79], v[120:123], v[104:107], v[64:79]\n\t" \
    "v_cvt_pk_bf16_f32 v96, v80, v81\n\t" \
    "v_cvt_pk_bf16_f32 v97, v82, v83\n\t" \
    "v_cvt_pk_bf16_f32 v98, v84, v85\n\t" \
    "v_cvt_pk_bf16_f32 v99, v86, v87\n\t" \
    "v_exp_f32 v90, v90\n\t" \
    "v_exp_f32 v91, v91\n\t" \
    "v_exp_f32 v92, v92\n\t" \
    "v_mfma_f32_32x32x16_bf16 v[16:31], v[124:127], v[96:99], v[16:31]\n\t" \
    "ds_read_b128 v[124:127], v138 offset:2560\n\t" \
    "v_exp_f32 v93, v93\n\t" \
    "v_exp_f32 v94, v94\n\t" \
    "v_exp_f32 v95, v95\n\t" \
    "v_cvt_pk_bf16_f32 v100, v88, v89\n\t" \
    "v_cvt_pk_bf16_f32 v101, v90, v91\n\t" \
    "v_cvt_pk_bf16_f32 v102, v92, v93\n\t" \
    "v_cvt_pk_bf16_f32 v103, v94, v95\n\t" \
    "v_exp_f32 v64, v64\n\t" \
    "v_exp_f32 v65, v65\n\t" \
    "v_mfma_f32_32x32x16_bf16 v[16:31], v[128:131], v[100:103], v[16:31]\n\t" \
    "ds_read_b128 v[128:131], v138 offset:3200\n\t" \
    "v_exp_f32 v66, v66\n\t" \
    "v_exp_f32 v67, v67\n\t" \
    "v_exp_f32 v68, v68\n\t" \
    "v_exp_f32 v69, v69\n\t" \
    "v_mfma_f32_32x32x8_bf16 v[80:95], v[132:133], v[116:117], v[48:63]\n\t" \
    "v_exp_f32 v70, v70\n\t" \
    "v_exp_f32 v71, v71\n\t" \
    "v_exp_f32 v72, v72\n\t" \
    "v_exp_f32 v73, v73\n\t" \
    "v_mfma_f32_32x32x16_bf16 v[80:95], v[120:123], v[108:111], v[80:95]\n\t" \
    "ds_read_b128 v[120:123], v136 offset:3072\n\t" \
    "v_cvt_pk_bf16_f32 v96, v64, v65\n\t" \
    "v_cvt_pk_bf16_f32 v97, v66, v67\n\t" \
    "v_cvt_pk_bf16_f32 v98, v68, v69\n\t" \
    "v_cvt_pk_bf16_f32 v99, v70, v71\n\t" \
    "v_exp_f32 v74, v74\n\t" \
    "v_exp_f32 v75, v75\n\t" \
    "v_exp_f32 v76, v76\n\t" \
    "s_waitcnt lgkmcnt(2)\n\t" \
    "v_mfma_f32_32x32x16_bf16 v[0:15], v[124:127], v[96:99], v[0:15]\n\t" \
    "v_exp_f32 v77, v77\n\t" \
    "v_exp_f32 v78, v78\n\t" \
    "v_exp_f32 v79, v79\n\t" \
    "v_cvt_pk_bf16_f32 v100, v72, v73\n\t" \
    "v_cvt_pk_bf16_f32 v101, v74, v75\n\t" \
    "v_cvt_pk_bf16_f32 v102, v76, v77\n\t" \
    "v_cvt_pk_bf16_f32 v103, v78, v79\n\t" \
    "s_waitcnt lgkmcnt(1)\n\t" \
    "v_exp_f32 v80, v80\n\t" \
    "v_exp_f32 v81, v81\n\t" \
    "v_mfma_f32_32x32x16_bf16 v[0:15], v[128:131], v[100:103], v[0:15]\n\t" \
    "v_exp_f32 v82, v82\n\t" \
    "v_exp_f32 v83, v83\n\t" \
    "v_exp_f32 v84, v84\n\t" \
    "v_exp_f32 v85, v85\n\t" \
    "s_waitcnt lgkmcnt(0)\n\t" \
    "v_mfma_f32_32x32x8_bf16 v[64:79], v[132:133], v[114:115], v[32:47]\n\t" \
    "v_exp_f32 v86, v86\n\t" \
    "v_exp_f32 v87, v87\n\t" \
    "v_exp_f32 v88, v88\n\t" \
    "v_exp_f32 v89, v89\n\t" \
    "v_mfma_f32_32x32x16_bf16 v[64:79], v[120:123], v[104:107], v[64:79]\n\t" \
    "v_cvt_pk_bf16_f32 v96, v80, v81\n\t" \
    "v_cvt_pk_bf16_f32 v97, v82, v83\n\t" \
    "v_cvt_pk_bf16_f32 v98, v84, v85\n\t" \
    "v_cvt_pk_bf16_f32 v99, v86, v87\n\t" \
    "v_exp_f32 v90, v90\n\t" \
    "v_exp_f32 v91, v91\n\t" \
    "v_exp_f32 v92, v92\n\t" \
    "v_mfma_f32_32x32x16_bf16 v[16:31], v[124:127], v[96:99], v[16:31]\n\t" \
    "ds_read_b128 v[124:127], v138 offset:3840\n\t" \
    "v_exp_f32 v93, v93\n\t" \
    "v_exp_f32 v94, v94\n\t" \
    "v_exp_f32 v95, v95\n\t" \
    "v_cvt_pk_bf16_f32 v100, v88, v89\n\t" \
    "v_cvt_pk_bf16_f32 v101, v90, v91\n\t" \
    "v_cvt_pk_bf16_f32 v102, v92, v93\n\t" \
    "v_cvt_pk_bf16_f32 v103, v94, v95\n\t" \
    "v_exp_f32 v64, v64\n\t" \
    "v_exp_f32 v65, v65\n\t" \
    "v_mfma_f32_32x32x16_bf16 v[16:31], v[128:131], v[100:103], v[16:31]\n\t" \
    "ds_read_b128 v[128:131], v138 offset:4480\n\t" \
    "v_exp_f32 v66, v66\n\t" \
    "v_exp_f32 v67, v67\n\t" \
    "v_exp_f32 v68, v68\n\t" \
    "v_exp_f32 v69, v69\n\t" \
    "v_mfma_f32_32x32x8_bf16 v[80:95], v[132:133], v[118:119], v[48:63]\n\t" \
    "v_exp_f32 v70, v70\n\t" \
    "v_exp_f32 v71, v71\n\t" \
    "v_exp_f32 v72, v72\n\t" \
    "v_exp_f32 v73, v73\n\t" \
    "v_mfma_f32_32x32x16_bf16 v[80:95], v[120:123], v[108:111], v[80:95]\n\t" \
    "ds_read_b128 v[120:123], v136 offset:9728\n\t" \
    "v_add_u32 v137, 9728, v137\n\t" \
    "ds_read2_b32 v[132:133], v137 offset0:0 offset1:32\n\t" \
    "v_cvt_pk_bf16_f32 v96, v64, v65\n\t" \
    "v_cvt_pk_bf16_f32 v97, v66, v67\n\t" \
    "v_cvt_pk_bf16_f32 v98, v68, v69\n\t" \
    "v_cvt_pk_bf16_f32 v99, v70, v71\n\t" \
    "v_exp_f32 v74, v74\n\t" \
    "v_exp_f32 v75, v75\n\t" \
    "v_exp_f32 v76, v76\n\t" \
    "s_waitcnt lgkmcnt(3)\n\t" \
    "v_mfma_f32_32x32x16_bf16 v[0:15], v[124:127], v[96:99], v[0:15]\n\t" \
    "v_exp_f32 v77, v77\n\t" \
    "v_exp_f32 v78, v78\n\t" \
    "v_exp_f32 v79, v79\n\t" \
    "v_cvt_pk_bf16_f32 v100, v72, v73\n\t" \
    "v_cvt_pk_bf16_f32 v101, v74, v75\n\t" \
    "v_cvt_pk_bf16_f32 v102, v76, v77\n\t" \
    "v_cvt_pk_bf16_f32 v103, v78, v79\n\t" \
    "s_waitcnt lgkmcnt(2)\n\t" \
    "v_exp_f32 v80, v80\n\t" \
    "v_exp_f32 v81, v81\n\t" \
    "v_mfma_f32_32x32x16_bf16 v[0:15], v[128:131], v[100:103], v[0:15]\n\t" \
    "v_exp_f32 v82, v82\n\t" \
    "v_exp_f32 v83, v83\n\t" \
    "v_exp_f32 v84, v84\n\t" \
    "v_exp_f32 v85, v85\n\t" \
    "s_waitcnt lgkmcnt(0)\n\t" \
    "v_mfma_f32_32x32x8_bf16 v[64:79], v[132:133], v[112:113], v[32:47]\n\t" \
    "v_exp_f32 v86, v86\n\t" \
    "v_exp_f32 v87, v87\n\t" \
    "v_exp_f32 v88, v88\n\t" \
    "v_exp_f32 v89, v89\n\t" \
    "v_mfma_f32_32x32x16_bf16 v[64:79], v[120:123], v[104:107], v[64:79]\n\t" \
    "v_cvt_pk_bf16_f32 v96, v80, v81\n\t" \
    "v_cvt_pk_bf16_f32 v97, v82, v83\n\t" \
    "v_cvt_pk_bf16_f32 v98, v84, v85\n\t" \
    "v_cvt_pk_bf16_f32 v99, v86, v87\n\t" \
    "v_exp_f32 v90, v90\n\t" \
    "v_exp_f32 v91, v91\n\t" \
    "v_exp_f32 v92, v92\n\t" \
    "v_mfma_f32_32x32x16_bf16 v[16:31], v[124:127], v[96:99], v[16:31]\n\t" \
    "ds_read_b128 v[124:127], v138 offset:9728\n\t" \
    "v_exp_f32 v93, v93\n\t" \
    "v_exp_f32 v94, v94\n\t" \
    "v_exp_f32 v95, v95\n\t" \
    "v_cvt_pk_bf16_f32 v100, v88, v89\n\t" \
    "v_cvt_pk_bf16_f32 v101, v90, v91\n\t" \
    "v_cvt_pk_bf16_f32 v102, v92, v93\n\t" \
    "v_cvt_pk_bf16_f32 v103, v94, v95\n\t" \
    "v_add_u32 v136, 9728, v136\n\t" \
    "v_add_u32 v138, 9728, v138\n\t" \
    "s_sub_u32 s42, s42, 1\n\t" \
    "s_cmp_lg_u32 s42, 0\n\t" \
    "s_cbranch_scc1 1b\n\t" \
    "v_mfma_f32_32x32x16_bf16 v[16:31], v[128:131], v[100:103], v[16:31]\n\t" \
    "s_waitcnt lgkmcnt(0)\n\t" \
    "s_nop 15\n\t"

DI void attn_phase(char* lds, const char* QM, const char* QX, const char* IMG, char* OM, char* OX, unsigned* cnt, unsigned id) {
    typedef __attribute__((address_space(3))) const u32x4* lptr;
    typedef __attribute__((address_space(3))) const unsigned* lptr1;
    int tid = threadIdx.x, bid = blockIdx.x;
    asm volatile("" : "+v"(tid)); asm volatile("" : "+s"(bid));
    const int lane = tid & 63, w = __builtin_amdgcn_readfirstlane(tid >> 6);
    const int r32 = lane & 31, hh = lane >> 5;
    int head, qb;
    { const int b = bid, xcd = b & 7, i = b >> 3; head = xcd * 4 + (i >> 3); qb = i & 7; }
    const int qg = w & 3, kh = w >> 2;
    const int qt0 = qb * 8 + qg * 2;
    wait_dep(cnt + (C_KV + (head >> 1)) * 64, 16, lds, id, w);

    u32x4 qm[2]; unsigned qx[2];
    const char* src = IMG + (size_t)head * IMG_HEAD;
    const unsigned piece0 = ((w & 1) * 76 + (w >> 1)) * 1024;
    const unsigned lb = (unsigned)(uintptr_t)lds + piece0;
    unsigned voff = lane * 16 + piece0;
    {
        const char* p0 = QM + ((size_t)head * 64 + qt0) * 1024;
        const char* p1 = QX + ((size_t)head * S_ + qt0 * 32) * 4;
        unsigned sv, cnt;
        asm volatile("global_load_dwordx4 %[q0], %[l16], %[p0]" SC1S "\n\tglobal_load_dwordx4 %[q1], %[l16], %[p0] offset:1024" SC1S "\n\t"
                     "global_load_dword %[x0], %[l4], %[p1]" SC1S "\n\tglobal_load_dword %[x1], %[l4], %[p1] offset:128" SC1S "\n\t"
                     "s_mov_b32 %[sv], m0\n\ts_mov_b32 m0, %[lb]\n\ts_mov_b32 %[cnt], 3\n\t"
                     "3:\n\t"
                     "global_load_lds_dwordx4 %[voff], %[src]" SC1S "\n\t"
                     "s_add_u32 m0, m0, 4096\n\t"
                     "v_add_u32 %[voff], 4096, %[voff]\n\t"
                     "s_sub_u32 %[cnt], %[cnt], 1\n\t"
                     "s_cmp_lg_u32 %[cnt], 0\n\t"
                     "s_cbranch_scc1 3b\n\t"
                     "s_mov_b32 m0, %[sv]"
                     : [q0] "=&v"(qm[0]), [q1] "=&v"(qm[1]), [x0] "=&v"(qx[0]), [x1] "=&v"(qx[1]), [voff] "+v"(voff), [sv] "=&s"(sv), [cnt] "=&s"(cnt)
                     : [l16] "v"(lane * 16), [l4] "v"(r32 * 4), [p0] "s"(p0), [p1] "s"(p1), [src] "s"(src), [lb] "s"(lb) : "memory", "scc");
    }
    const char* gbase = lds + (size_t)kh * 8 * IMG_GROUP;
    const int offK = lane * 16, offX = 4096 + r32 * 4, offV = 4608 + hh * VT_CHUNK + vpos(r32) * 16;
    const unsigned gb0 = (unsigned)(uintptr_t)gbase;
    f32x16 negm[2], o[2];
    float mrow[2];
    asm volatile("s_waitcnt vmcnt(0)\n\ts_barrier" : "+v"(qm[0]), "+v"(qm[1]), "+v"(qx[0]), "+v"(qx[1]) :: "memory");

    bf16x8 qf[2]; bf16x4 qx0[2], qx1[2];
#pragma unroll
    for (int t = 0; t < 2; ++t) {
        qf[t] = asbf8(qm[t]);
        const unsigned e = hh ? 0u : qx[t];
        qx0[t] = asbf4((u32x2){e, 0u});
        qx1[t] = asbf4((u32x2){0u, e});
    }
    {
        const unsigned kx = *(lptr1)(gbase + offX);
        const bf16x8 km = asbf8(*(lptr)(gbase + offK));
        const f32x16 z = splat16(0.f);
#pragma unroll
        for (int t = 0; t < 2; ++t) {
            f32x16 s = mfma32k8(asbf4((u32x2){kx, kx}), qx0[t], z);
            s = mfma32(km, qf[t], s);
            mrow[t] = xhalf_max(max16(s));
        }
    }
#pragma unroll
    for (int t = 0; t < 2; ++t) { negm[t] = splat16(-mrow[t]); o[t] = splat16(0.f); }
    {
        unsigned aK = gb0 + offK, aX = gb0 + offX, aV = gb0 + offV;
        const char* sp = src;
        asm volatile(ATTN_LOOP_ASM
            : "+{v[0:15]}"(o[0]), "+{v[16:31]}"(o[1]), "+{v136}"(aK), "+{v137}"(aX), "+{v138}"(aV),
              "+{s[40:41]}"(sp)
            : "{v[32:47]}"(negm[0]), "{v[48:63]}"(negm[1]), "{v[104:107]}"(qf[0]), "{v[108:111]}"(qf[1]),
              "{v[112:113]}"(qx0[0]), "{v[114:115]}"(qx1[0]), "{v[116:117]}"(qx0[1]), "{v[118:119]}"(qx1[1]),
              [voff] "v"(voff), [lb] "s"(lb + 3 * 4096)
            : "memory", "scc", "s42",
              "v64","v65","v66","v67","v68","v69","v70","v71","v72","v73","v74","v75","v76","v77","v78","v79",
              "v80","v81","v82","v83","v84","v85","v86","v87","v88","v89","v90","v91","v92","v93","v94","v95",
              "v96","v97","v98","v99","v100","v101","v102","v103",
              "v120","v121","v122","v123","v124","v125","v126","v127","v128","v129","v130","v131","v132","v133");
    }
    {
        float osum = 0.f;
#pragma unroll
        for (int i = 0; i < 10; ++i) osum += fabsf(o[0][i]) + fabsf(o[1][i]);
        const bool bad = !(o[0][10] < 1.2676506e30f) || !(o[1][10] < 1.2676506e30f) || !(osum < 3.0e38f);
        if (__builtin_expect(__any(bad), 0)) {
            const char* gg = src + (size_t)kh * 8 * IMG_GROUP;
            const f32x16 z = splat16(0.f);
            float mx[2] = {-INFINITY, -INFINITY};
#pragma unroll 1
            for (int pass = 0; pass < 2; ++pass) {
                o[0] = z; o[1] = z;
#pragma unroll 1
                for (int kt = 0; kt < 32; ++kt) {
                    const char* gb = gg + (kt >> 2) * IMG_GROUP;
                    unsigned kx; u32x4 km, v0, v1;
                    asm volatile("global_load_dword %0, %4, off sc1\n\tglobal_load_dwordx4 %1, %5, off sc1\n\t"
                                 "global_load_dwordx4 %2, %6, off sc1\n\tglobal_load_dwordx4 %3, %6, off offset:640 sc1\n\ts_waitcnt vmcnt(0)"
                                 : "=&v"(kx), "=&v"(km), "=&v"(v0), "=&v"(v1)
                                 : "v"(gb + (kt & 3) * 128 + offX), "v"(gb + (kt & 3) * 1024 + offK), "v"(gb + (kt & 3) * VT_TILE + offV) : "memory");
#pragma unroll
                    for (int t = 0; t < 2; ++t) {
                        f32x16 s = mfma32k8(asbf4((u32x2){kx, kx}), qx0[t], z);
                        s = mfma32(asbf8(km), qf[t], s);
                        mx[t] = fmaxf(mx[t], max16(s));
                        u32x4 p0, p1;
#pragma unroll
                        for (int j = 0; j < 4; ++j) {
                            p0[j] = pk2(__builtin_amdgcn_exp2f(s[2 * j] - mrow[t]), __builtin_amdgcn_exp2f(s[2 * j + 1] - mrow[t]));
                            p1[j] = pk2(__builtin_amdgcn_exp2f(s[8 + 2 * j] - mrow[t]), __builtin_amdgcn_exp2f(s[9 + 2 * j] - mrow[t]));
                        }
                        o[t] = mfma32(asbf8(v0), asbf8(p0), o[t]);
                        o[t] = mfma32(asbf8(v1), asbf8(p1), o[t]);
                    }
                }
                mrow[0] = xhalf_max(mx[0]); mrow[1] = xhalf_max(mx[1]);
            }
        }
    }
    {
        float* dst = (float*)(lds + kh * (8 * IMG_GROUP)) + (size_t)(qg * 12) * 64 + lane;
#pragma unroll
        for (int i = 0; i < 11; ++i) dst[i * 64] = kh ? o[0][i] : o[1][i];
        dst[11 * 64] = kh ? mrow[0] : mrow[1];
    }
    __syncthreads();
    {
        const float* src = (const float*)(lds + (1 - kh) * (8 * IMG_GROUP)) + (size_t)(qg * 12) * 64 + lane;
        const float mo = kh ? mrow[1] : mrow[0];
        const float mp = src[11 * 64];
        const float mm = fmaxf(mo, mp);
        float fo = __builtin_amdgcn_exp2f(mo - mm), fp = __builtin_amdgcn_exp2f(mp - mm);
        const float rl = __builtin_amdgcn_rcpf((kh ? o[1][10] : o[0][10]) * fo + src[10 * 64] * fp);
        fo *= rl; fp *= rl;
        float v[10];
#pragma unroll
        for (int i = 0; i < 10; ++i) v[i] = (kh ? o[1][i] : o[0][i]) * fo + src[i * 64] * fp;
        const int q = (qt0 + kh) * 32 + r32, st = q >> 4;
        const u32x4 m8 = {pk2(v[0], v[1]), pk2(v[2], v[3]), pk2(v[4], v[5]), pk2(v[6], v[7])};
        st16(OM + ((size_t)st * 16 + (head >> 1)) * 1024 + ((2 * (head & 1) + hh) * 16 + (q & 15)) * 16, m8);
        if (hh == 0)
            st4(OX + ((size_t)st * 2 + (head >> 4)) * 1024 + ((head & 3) * 16 + (q & 15)) * 16 + ((head >> 2) & 3) * 4, pk2(v[8], v[9]));
    }
    drain_and_sync();
    if (tid == 0) cnt_add(cnt + (C_O + qb) * 64);
}

__global__ __launch_bounds__(512, 2) void mega_kernel(char* __restrict__ ws, uint16_t* __restrict__ out) {
    __shared__ __attribute__((aligned(16))) char lds[LDS_BYTES];
    if (threadIdx.x < 16) *(ldsflag_t)(lds + LDS_FLAG + threadIdx.x * 4) = 0u;
    __syncthreads();
#pragma unroll 1
    for (int l = 0; l < 2; ++l) {
        char* L = ws + OFF_L + (size_t)l * L_BYTES;
        char* X0 = ws + OFF_XT + (size_t)(2 * l) * AL(XT_BYTES);
        char* X1 = X0 + AL(XT_BYTES);
        char* X2 = X1 + AL(XT_BYTES);
        char* S0 = ws + OFF_ST + (size_t)(2 * l) * AL(ST_BYTES);
        char* S1 = S0 + AL(ST_BYTES);
        char* S2 = S1 + AL(ST_BYTES);
        float* KV = (float*)(ws + OFF_KVB + (size_t)l * AL(KVB_BYTES));
        unsigned* cnt = (unsigned*)(ws + OFF_CNT) + l * (96 * 64);
        const unsigned id = l * 6 + 1;
        qkv_phase<true>(lds, X0, S0, ws + OFF_WTA, L + L_PM, L + L_PX, L + L_IMG, KV, l ? cnt - 96 * 64 + C_X2 * 64 : nullptr, cnt, id, (2 * l + 1) * 512);
        oproj_phase<true>(lds, L + L_PM, L + L_PX, KV, ws + OFF_WOA, X0, X1, (f32x2*)S1, out, false, cnt, id + 1);
        qkv_phase<false>(lds, X1, S1, ws + OFF_WTB, L + L_QM, L + L_QX, L + L_IMG, KV, cnt + C_X1 * 64, cnt, id + 3, (2 * l + 2) * 512);
        attn_phase(lds, L + L_QM, L + L_QX, L + L_IMG, L + L_OM, L + L_OX, cnt, id + 4);
        oproj_phase<false>(lds, L + L_OM, L + L_OX, KV, ws + OFF_WOB, X1, X2, (f32x2*)S2, out, l == 1, cnt, id + 5);
    }
}

extern "C" void kernel_launch(void* const* d_in, const int* in_sizes, int n_in,
                              void* d_out, int out_size, void* d_ws, size_t ws_size,
                              hipStream_t stream) {
    const uint16_t* x0  = (const uint16_t*)d_in[0];
    const uint16_t* wA  = (const uint16_t*)d_in[1];
    const uint16_t* woA = (const uint16_t*)d_in[2];
    const uint16_t* wB  = (const uint16_t*)d_in[4];
    const uint16_t* woB = (const uint16_t*)d_in[5];
    char* ws = (char*)d_ws;
    prep_kernel<<<PREP_W_BLOCKS + PREP_WO_BLOCKS, 832, 0, stream>>>(x0, wA, woA, wB, woB, ws);
    mega_kernel<<<256, 512, 0, stream>>>(ws, (uint16_t*)d_out);
}
```
